```python
import torch

def _softmax(x, axis=-1):
    return torch.softmax(x, dim=axis)

def _gelu(x):
    return torch.nn.functional.gelu(x, approximate='tanh')

def _silu(x):
    return torch.nn.functional.silu(x)

def _sigmoid(x):
    return torch.sigmoid(x)

def _layernorm(x, axis=-1, eps=1e-05):
    m = x.mean(dim=axis, keepdim=True)
    v = x.var(dim=axis, keepdim=True, unbiased=False)
    return (x - m) / torch.sqrt(v + eps)

def _scatter_add(tgt, idx, src, axis=0):
    return tgt.clone().index_add_(axis, idx, src)

def _segment_sum(src, seg, n):
    out = torch.zeros((n,) + src.shape[1:], dtype=src.dtype)
    return out.index_add_(0, seg, src)

def _pad(x, pw):
    flat = tuple((v for pair in reversed(pw) for v in pair))
    return torch.nn.functional.pad(x, flat)
kernel_name = 'adaLN_cond__mha_block_cached__mixer_gmlp__prompt'

def setup_inputs():

    _w = lambda a: (lambda t: t.to(torch.bfloat16) if t.is_floating_point() else t)(torch.as_tensor(a))

    def _A_setup():
        B, Sq, D = (1, 2048, 512)
        H, Sk, Dh = (32, 2048, 128)
        return {'x': _w((lambda *s: torch.randn(*s, dtype=torch.bfloat16) * 0.05)(B, Sq, D)), 'k_cache': _w((lambda *s: torch.randn(*s, dtype=torch.bfloat16) * 3.2)(B, H, Sk, Dh)), 'v_cache': _w((lambda *s: torch.randn(*s, dtype=torch.bfloat16) * 0.4)(B, H, Sk, Dh)), 'wq': _w((lambda *s: torch.randn(*s, dtype=torch.bfloat16) * 3.2)(D, H * Dh) * D ** (-0.5)), 'wo': _w((lambda *s: torch.randn(*s, dtype=torch.bfloat16) * 0.4)(H * Dh, D) * (H * Dh) ** (-0.5))}
    _A_in = _A_setup()

    def _B_setup():
        B, S, D, F = (1, 2048, 512, 1792)
        Fh = F // 2
        return {'x': _w((lambda *s: torch.randn(*s, dtype=torch.bfloat16) * 0.05)(B, S, D)), 'w_in': _w((lambda *s: torch.randn(*s, dtype=torch.bfloat16) * 0.8)(D, 2 * Fh)), 'w_sp': _w(0.01 * (lambda *s: torch.randn(*s, dtype=torch.bfloat16) * 0.8)(S, S)), 'b_sp': _w(1.0 + 0.01 * (lambda *s: torch.randn(*s, dtype=torch.bfloat16) * 0.05)(S)), 'w_out': _w((lambda *s: torch.randn(*s, dtype=torch.bfloat16) * 0.05)(Fh, D))}
    _B_in = _B_setup()
    return {'A_x': _A_in['x'], 'A_k_cache': _A_in['k_cache'], 'A_v_cache': _A_in['v_cache'], 'A_wq': _A_in['wq'], 'A_wo': _A_in['wo'], 'B_x': _B_in['x'], 'B_w_in': _B_in['w_in'], 'B_w_sp': _B_in['w_sp'], 'B_b_sp': _B_in['b_sp'], 'B_w_out': _B_in['w_out'], 'c': _w((lambda *s: torch.randn(*s, dtype=torch.bfloat16) * 0.1)(1, 512) * 8.0), 'w_mod': _w((lambda *s: torch.randn(*s, dtype=torch.bfloat16) * 0.1)(512, 6 * 512) * 512 ** (-0.5) * 8.0)}

def reference(A_x, A_k_cache, A_v_cache, A_wq, A_wo, B_x, B_w_in, B_w_sp, B_b_sp, B_w_out, c, w_mod):

    def _A_ref(x, k_cache, v_cache, wq, wo):
        B, Sq, D = x.shape
        H, Dh = (32, 128)
        q = (lambda x, *d: x.permute(*d))((x @ wq).reshape(B, Sq, H, Dh), 0, 2, 1, 3)
        scores = torch.einsum('bhqd,bhkd->bhqk', q, k_cache) * Dh ** (-0.5)
        p = _softmax(scores, axis=-1)
        o = torch.einsum('bhqk,bhkd->bhqd', p, v_cache)
        return (lambda x, *d: x.permute(*d))(o, 0, 2, 1, 3).reshape(B, Sq, H * Dh) @ wo

    def _B_ref(x, w_in, w_sp, b_sp, w_out):
        B, S, D = x.shape
        F = w_in.shape[-1]
        Fh = F // 2
        z = _gelu(torch.einsum('bsd,df->bsf', x, w_in))
        z1 = z[..., :Fh].clone()
        z2 = _layernorm(z[..., Fh:], axis=-1)
        g = torch.einsum('ts,bsf->btf', w_sp, z2) + b_sp[None, :, None]
        return torch.einsum('bsf,fd->bsd', z1 * g, w_out)
    x = A_x
    mod = (_silu(c) @ w_mod).reshape(c.shape[0], 6, 1, -1)
    sa, ca, ga, sb, cb, gb = (mod[:, 0].clone(), mod[:, 1].clone(), mod[:, 2].clone(), mod[:, 3].clone(), mod[:, 4].clone(), mod[:, 5].clone())
    h = _layernorm(x, axis=-1) * (1 + ca) + sa
    x = x + ga * _A_ref(h, A_k_cache, A_v_cache, A_wq, A_wo)
    h = _layernorm(x, axis=-1) * (1 + cb) + sb
    x = x + gb * _B_ref(h, B_w_in, B_w_sp, B_b_sp, B_w_out)
    return x
```

```cpp
#include <hip/hip_runtime.h>
#include <hip/hip_bf16.h>
#include <stdint.h>

typedef unsigned short u16;
typedef __attribute__((ext_vector_type(8))) short bf16x8;
typedef __attribute__((ext_vector_type(4))) short s16x4;
typedef __attribute__((ext_vector_type(16))) float f32x16;
typedef __attribute__((ext_vector_type(4))) unsigned u32x4;

constexpr int S_ = 2048, D_ = 512, H_ = 32, DH = 128, HD = H_ * DH;
constexpr int F_ = 1792, FH = 896;

__device__ __forceinline__ float bf2f(u16 v) { return __uint_as_float((unsigned)v << 16); }
__device__ __forceinline__ u16 f2bf(float f) {
  unsigned u = __float_as_uint(f);
  u += 0x7fffu + ((u >> 16) & 1u);
  return (u16)(u >> 16);
}
__device__ __forceinline__ float rbf(float f) { return bf2f(f2bf(f)); }
__device__ __forceinline__ unsigned cvtpk(float lo, float hi) {
  unsigned r; asm volatile("v_cvt_pk_bf16_f32 %0, %1, %2" : "=v"(r) : "v"(lo), "v"(hi)); return r;
}
__device__ __forceinline__ int crow(int r, int hi) { return (r & 3) + 8 * (r >> 2) + 4 * hi; }
typedef __attribute__((ext_vector_type(4))) __bf16 bfv4;
typedef __attribute__((address_space(3))) bfv4 lds_bfv4;
__device__ __forceinline__ s16x4 tr_read_b(const void* lds_ptr) {
  bfv4 v = __builtin_amdgcn_ds_read_tr16_b64_v4bf16((lds_bfv4*)(lds_ptr));
  return *reinterpret_cast<s16x4*>(&v);
}
__device__ __forceinline__ float wave_sum(float v) {
#pragma unroll
  for (int o = 32; o >= 1; o >>= 1) v += __shfl_xor(v, o, 64);
  return v;
}

__global__ __launch_bounds__(256) void k_mod(const u16* __restrict__ c, const u16* __restrict__ w_mod, float* __restrict__ mod) {
  __shared__ float red[4][64];
  const int lane = threadIdx.x & 63, w = threadIdx.x >> 6;
  const int col = blockIdx.x * 64 + lane;
  float acc = 0.f;
  for (int k = w * 128; k < w * 128 + 128; ++k) {
    float cv = bf2f(c[k]);
    float sv = rbf(cv / (1.f + __expf(-cv)));
    acc += sv * bf2f(w_mod[(long)k * 3072 + col]);
  }
  red[w][lane] = acc;
  __syncthreads();
  if (w == 0) mod[col] = rbf(red[0][lane] + red[1][lane] + red[2][lane] + red[3][lane]);
}

__global__ __launch_bounds__(256) void k_ln1(const u16* __restrict__ x, const float* __restrict__ mod, u16* __restrict__ h) {
  const int lane = threadIdx.x & 63, row = blockIdx.x * 4 + (threadIdx.x >> 6);
  bf16x8 xv = *reinterpret_cast<const bf16x8*>(x + (long)row * D_ + lane * 8);
  float v[8]; float s = 0.f;
#pragma unroll
  for (int j = 0; j < 8; ++j) { v[j] = bf2f((u16)xv[j]); s += v[j]; }
  float mean = wave_sum(s) * (1.f / D_);
  float q = 0.f;
#pragma unroll
  for (int j = 0; j < 8; ++j) { float d = v[j] - mean; q += d * d; }
  float rstd = rsqrtf(wave_sum(q) * (1.f / D_) + 1e-5f);
  bf16x8 o;
#pragma unroll
  for (int j = 0; j < 8; ++j) {
    int cidx = lane * 8 + j;
    o[j] = (short)f2bf((v[j] - mean) * rstd * (1.f + mod[D_ + cidx]) + mod[cidx]);
  }
  *reinterpret_cast<bf16x8*>(h + (long)row * D_ + lane * 8) = o;
}

enum { EPI_BF16 = 0, EPI_F32 = 1, EPI_GELU = 2, EPI_SP = 3, EPI_OUT = 4 };
struct EpiArgs {
  void* C;
  const u16* aux0;
  const u16* aux1;
  const float* auxf;
  int ldc;
  int pad;
};

__device__ __forceinline__ float gelu_tanh(float x) {
  const float k0 = 0.7978845608028654f, k1 = 0.044715f;
  float u = k0 * (x + k1 * x * x * x);
  float e = __expf(2.f * u);
  float t = 1.f - 2.f / (e + 1.f);
  return 0.5f * x * (1.f + t);
}

template <int BM, int BN, int WM, int WN, int EPI>
__global__ __launch_bounds__(WM * WN * 64) void k_gemm(const u16* __restrict__ A, const u16* __restrict__ B, EpiArgs ea,
                                                      int lda, int ldb, int K, int ksplit) {
  constexpr int BK = 64, NT = WM * WN * 64;
  constexpr int TM = BM / WM / 32, TN = BN / WN / 32;
  constexpr int ACH = BM * 8 / NT, BCH = BN * 8 / NT;
  constexpr int ABYTES = BM * BK * 2, BBYTES = BK * BN * 2;
  extern __shared__ __attribute__((aligned(16))) char smem[];
  const int tid = threadIdx.x, lane = tid & 63, wid = tid >> 6;
  const int wm = wid / WN, wn = wid % WN;
  const int r32 = lane & 31, hi = lane >> 5;
  const int bm = blockIdx.y * BM, bn = blockIdx.x * BN;
  const int kbeg = blockIdx.z * ksplit;
  const int nt = ksplit / BK;
  A += (long)bm * lda + kbeg;
  B += (long)kbeg * ldb + bn;

  bf16x8 ra[ACH], rb[BCH];
  auto gload = [&](int t) {
#pragma unroll
    for (int i = 0; i < ACH; ++i) {
      int c = tid + i * NT; int row = c >> 3, ch = c & 7;
      ra[i] = *reinterpret_cast<const bf16x8*>(A + (long)row * lda + t * BK + ch * 8);
    }
#pragma unroll
    for (int i = 0; i < BCH; ++i) {
      int c = tid + i * NT; int row = c / (BN / 8), ch = c % (BN / 8);
      rb[i] = *reinterpret_cast<const bf16x8*>(B + (long)(t * BK + row) * ldb + ch * 8);
    }
  };
  auto swrite = [&](int buf) {
    char* sa = smem + buf * (ABYTES + BBYTES);
    char* sb = sa + ABYTES;
#pragma unroll
    for (int i = 0; i < ACH; ++i) {
      int c = tid + i * NT; int row = c >> 3, ch = c & 7;
      *reinterpret_cast<bf16x8*>(sa + row * 128 + ((ch ^ ((row >> 1) & 7)) << 4)) = ra[i];
    }
#pragma unroll
    for (int i = 0; i < BCH; ++i) {
      int c = tid + i * NT; int row = c / (BN / 8), ch = c % (BN / 8);
      *reinterpret_cast<bf16x8*>(sb + (ch >> 2) * 4096 + row * 64 + (ch & 3) * 16) = rb[i];
    }
  };
  f32x16 acc[TM][TN];
#pragma unroll
  for (int m = 0; m < TM; ++m)
#pragma unroll
    for (int n = 0; n < TN; ++n) acc[m][n] = f32x16{};

  gload(0); swrite(0); __syncthreads();
  const int q4 = (lane & 15) >> 2, p4 = lane & 3, blk = (lane >> 4) & 1;
  for (int t = 0; t < nt; ++t) {
    const int buf = t & 1;
    if (t + 1 < nt) gload(t + 1);
    const char* sa = smem + buf * (ABYTES + BBYTES);
    const char* sb = sa + ABYTES;
#pragma unroll
    for (int s = 0; s < 4; ++s) {
      bf16x8 af[TM], bfr[TN];
#pragma unroll
      for (int m = 0; m < TM; ++m) {
        int row = (wm * TM + m) * 32 + r32; int ch = 2 * s + hi;
        af[m] = *reinterpret_cast<const bf16x8*>(sa + row * 128 + ((ch ^ ((row >> 1) & 7)) << 4));
      }
#pragma unroll
      for (int n = 0; n < TN; ++n) {
        const char* pb = sb + (wn * TN + n) * 4096 + (16 * s + 8 * hi + q4) * 64 + blk * 32 + p4 * 8;
        s16x4 lo = tr_read_b(pb), hh = tr_read_b(pb + 256);
        bfr[n] = (bf16x8){lo[0], lo[1], lo[2], lo[3], hh[0], hh[1], hh[2], hh[3]};
      }
#pragma unroll
      for (int m = 0; m < TM; ++m)
#pragma unroll
        for (int n = 0; n < TN; ++n)
          acc[m][n] = __builtin_amdgcn_mfma_f32_32x32x16_bf16(af[m], bfr[n], acc[m][n], 0, 0, 0);
    }
    if (t + 1 < nt) swrite(buf ^ 1);
    __syncthreads();
  }
#pragma unroll
  for (int m = 0; m < TM; ++m)
#pragma unroll
    for (int n = 0; n < TN; ++n) {
      const int col = bn + (wn * TN + n) * 32 + r32;
#pragma unroll
      for (int r = 0; r < 16; ++r) {
        const int row = bm + (wm * TM + m) * 32 + crow(r, hi);
        float v = acc[m][n][r];
        if constexpr (EPI == EPI_BF16) {
          ((u16*)ea.C)[(long)row * ea.ldc + col] = f2bf(v);
        } else if constexpr (EPI == EPI_F32) {
          ((float*)ea.C)[((long)blockIdx.z * S_ + row) * ea.ldc + col] = v;
        } else if constexpr (EPI == EPI_GELU) {
          ((u16*)ea.C)[(long)row * ea.ldc + col] = f2bf(gelu_tanh(rbf(v)));
        } else if constexpr (EPI == EPI_SP) {
          float g = rbf(rbf(v) + bf2f(ea.aux1[row]));
          float z1 = bf2f(ea.aux0[(long)row * F_ + col]);
          ((u16*)ea.C)[(long)row * ea.ldc + col] = f2bf(z1 * g);
        } else {
          float x1 = bf2f(ea.aux0[(long)row * D_ + col]);
          ((u16*)ea.C)[(long)row * ea.ldc + col] = f2bf(x1 + rbf(ea.auxf[col] * rbf(v)));
        }
      }
    }
}

template <int NSPLIT>
__global__ __launch_bounds__(256) void k_resid_ln2(const u16* __restrict__ x, const float* __restrict__ part, const float* __restrict__ mod,
                                                   u16* __restrict__ x1o, u16* __restrict__ h2) {
  const int lane = threadIdx.x & 63, row = blockIdx.x * 4 + (threadIdx.x >> 6);
  bf16x8 xv = *reinterpret_cast<const bf16x8*>(x + (long)row * D_ + lane * 8);
  float a[8];
#pragma unroll
  for (int j = 0; j < 8; ++j) a[j] = 0.f;
#pragma unroll
  for (int p = 0; p < NSPLIT; ++p) {
    const float4* pp = reinterpret_cast<const float4*>(part + ((long)p * S_ + row) * D_ + lane * 8);
    float4 u0 = pp[0], u1 = pp[1];
    a[0] += u0.x; a[1] += u0.y; a[2] += u0.z; a[3] += u0.w; a[4] += u1.x; a[5] += u1.y; a[6] += u1.z; a[7] += u1.w;
  }
  float v[8]; float s = 0.f; bf16x8 xo;
#pragma unroll
  for (int j = 0; j < 8; ++j) {
    int cidx = lane * 8 + j;
    float t = rbf(bf2f((u16)xv[j]) + rbf(mod[2 * D_ + cidx] * rbf(a[j])));
    v[j] = t; s += t; xo[j] = (short)f2bf(t);
  }
  *reinterpret_cast<bf16x8*>(x1o + (long)row * D_ + lane * 8) = xo;
  float mean = wave_sum(s) * (1.f / D_);
  float q = 0.f;
#pragma unroll
  for (int j = 0; j < 8; ++j) { float d = v[j] - mean; q += d * d; }
  float rstd = rsqrtf(wave_sum(q) * (1.f / D_) + 1e-5f);
  bf16x8 o;
#pragma unroll
  for (int j = 0; j < 8; ++j) {
    int cidx = lane * 8 + j;
    o[j] = (short)f2bf((v[j] - mean) * rstd * (1.f + mod[4 * D_ + cidx]) + mod[3 * D_ + cidx]);
  }
  *reinterpret_cast<bf16x8*>(h2 + (long)row * D_ + lane * 8) = o;
}

__global__ __launch_bounds__(256) void k_ln_z2(const u16* __restrict__ z, u16* __restrict__ z2n) {
  const int lane = threadIdx.x & 63, row = blockIdx.x * 4 + (threadIdx.x >> 6);
  float v[16]; float s = 0.f; const bool act = lane < 56;
  bf16x8 c0 = {}, c1 = {};
  if (act) {
    c0 = *reinterpret_cast<const bf16x8*>(z + (long)row * F_ + FH + lane * 16);
    c1 = *reinterpret_cast<const bf16x8*>(z + (long)row * F_ + FH + lane * 16 + 8);
  }
#pragma unroll
  for (int j = 0; j < 8; ++j) { v[j] = bf2f((u16)c0[j]); v[8 + j] = bf2f((u16)c1[j]); s += v[j] + v[8 + j]; }
  float mean = wave_sum(s) * (1.f / FH);
  float q = 0.f;
  if (act) {
#pragma unroll
    for (int j = 0; j < 16; ++j) { float d = v[j] - mean; q += d * d; }
  }
  float rstd = rsqrtf(wave_sum(q) * (1.f / FH) + 1e-5f);
  if (act) {
    bf16x8 o0, o1;
#pragma unroll
    for (int j = 0; j < 8; ++j) { o0[j] = (short)f2bf((v[j] - mean) * rstd); o1[j] = (short)f2bf((v[8 + j] - mean) * rstd); }
    *reinterpret_cast<bf16x8*>(z2n + (long)row * FH + lane * 16) = o0;
    *reinterpret_cast<bf16x8*>(z2n + (long)row * FH + lane * 16 + 8) = o1;
  }
}

constexpr int NW = 8, QBLK = 32, KVBLK = 64;
constexpr float SCALE = 0.088388347648318440f;
constexpr float THR = 8.f;
constexpr size_t SHM_V = KVBLK * DH * 2, SHM_K = KVBLK * DH * 2, SHM_ATTN = 2 * SHM_V + 2 * SHM_K + NW * 64 * 4;
#define KSWZ(row, colB) ((row) * 256 + ((colB) ^ (((row) & 7) << 4)))
#define SBAR() __builtin_amdgcn_sched_barrier(0)

__device__ __forceinline__ void partialSM(f32x16& p0, f32x16& p1, float& m_reg, float& mn, float& alpha) {
  constexpr float C = SCALE * 1.4426950408889634f;
  float pmax = p0[0];
  for (int r = 1; r < 16; ++r) pmax = fmaxf(pmax, p0[r]);
  for (int r = 0; r < 16; ++r) pmax = fmaxf(pmax, p1[r]);
  { auto rr = __builtin_amdgcn_permlane32_swap(__float_as_uint(pmax), __float_as_uint(pmax), false, false);
    pmax = fmaxf(__uint_as_float(rr[0]), __uint_as_float(rr[1])); }
  if (__builtin_expect(__all(pmax - m_reg <= THR / SCALE), 1)) { mn = m_reg; alpha = 1.f; }
  else { mn = fmaxf(m_reg, pmax); alpha = __builtin_amdgcn_exp2f((m_reg - mn) * C); m_reg = mn; }
  float mnC = -mn * C;
  for (int r = 0; r < 16; ++r) p0[r] = fmaf(p0[r], C, mnC);
  for (int r = 0; r < 16; ++r) p1[r] = fmaf(p1[r], C, mnC);
  for (int r = 0; r < 16; ++r) p0[r] = __builtin_amdgcn_exp2f(p0[r]);
}
__device__ __forceinline__ void finishSM(f32x16& p0, f32x16& p1, float alpha, float& l_reg, bf16x8& pa0, bf16x8& pa1, bf16x8& pa2, bf16x8& pa3) {
  for (int r = 0; r < 16; ++r) p1[r] = __builtin_amdgcn_exp2f(p1[r]);
  float ps = 0;
  for (int r = 0; r < 16; ++r) ps += p0[r];
  for (int r = 0; r < 16; ++r) ps += p1[r];
  { auto rr = __builtin_amdgcn_permlane32_swap(__float_as_uint(ps), __float_as_uint(ps), false, false);
    ps = __uint_as_float(rr[0]) + __uint_as_float(rr[1]); }
  l_reg = l_reg * alpha + ps;
#define PK4(P, BASE, OUT) do { unsigned a0 = cvtpk(P[BASE + 0], P[BASE + 1]), a1 = cvtpk(P[BASE + 2], P[BASE + 3]);   \
    unsigned b0 = cvtpk(P[BASE + 4], P[BASE + 5]), b1 = cvtpk(P[BASE + 6], P[BASE + 7]);                              \
    auto r0 = __builtin_amdgcn_permlane32_swap(a0, b0, false, false); auto r1 = __builtin_amdgcn_permlane32_swap(a1, b1, false, false); \
    u32x4 w = {r0[0], r1[0], r0[1], r1[1]}; OUT = *reinterpret_cast<bf16x8*>(&w); } while (0)
  PK4(p0, 0, pa0); PK4(p0, 8, pa1); PK4(p1, 0, pa2); PK4(p1, 8, pa3);
#undef PK4
}
__device__ __forceinline__ void qkt(f32x16& p0, f32x16& p1, const u16* Ks, const bf16x8* qr, int r32, int hi) {
  p0 = f32x16{}; p1 = f32x16{};
  for (int d0 = 0; d0 < 8; ++d0) { int cb = (d0 * 16 + hi * 8) * 2;
    bf16x8 b0 = *reinterpret_cast<const bf16x8*>((const char*)Ks + KSWZ(r32, cb));
    bf16x8 b1 = *reinterpret_cast<const bf16x8*>((const char*)Ks + KSWZ(32 + r32, cb));
    p0 = __builtin_amdgcn_mfma_f32_32x32x16_bf16(b0, qr[d0], p0, 0, 0, 0);
    p1 = __builtin_amdgcn_mfma_f32_32x32x16_bf16(b1, qr[d0], p1, 0, 0, 0); }
}
__device__ __forceinline__ int v_st(int k, int c) { const int kk = (k & ~0xC) | ((k & 4) << 1) | ((k & 8) >> 1); return ((kk >> 3) * 4 + (c >> 5)) * 512 + ((kk & 7) * 32 + (c & 31)) * 2; }
__device__ __forceinline__ int v_rd_base(int lane) { return ((lane & 3) << 3) | (((lane >> 2) & 3) << 6) | (((lane >> 4) & 1) << 5) | (((lane >> 5) & 1) << 8); }
constexpr int v_rd_off(int d0, int ks, int half) { return d0 * 512 + ks * 4096 + half * 2048; }
template <int OFF> __device__ __forceinline__ s16x4 tr_read(int vb) {
  s16x4 r; asm volatile("ds_read_b64_tr_b16 %0, %1 offset:%2" : "=&v"(r) : "v"(vb), "i"(OFF) : "memory"); return r;
}
template <int D0> __device__ __forceinline__ void pv_one(f32x16& od, int vb, bf16x8 pa0, bf16x8 pa1, bf16x8 pa2, bf16x8 pa3) {
  const s16x4 l0 = tr_read<v_rd_off(D0, 0, 0)>(vb), h0 = tr_read<v_rd_off(D0, 0, 1)>(vb), l1 = tr_read<v_rd_off(D0, 1, 0)>(vb), h1 = tr_read<v_rd_off(D0, 1, 1)>(vb);
  const s16x4 l2 = tr_read<v_rd_off(D0, 2, 0)>(vb), h2 = tr_read<v_rd_off(D0, 2, 1)>(vb), l3 = tr_read<v_rd_off(D0, 3, 0)>(vb), h3 = tr_read<v_rd_off(D0, 3, 1)>(vb);
  asm volatile("s_waitcnt lgkmcnt(0)" ::: "memory"); SBAR();
#define PK(L, H) (bf16x8){L[0], L[1], L[2], L[3], H[0], H[1], H[2], H[3]}
  od = __builtin_amdgcn_mfma_f32_32x32x16_bf16(pa0, PK(l0, h0), od, 0, 0, 0);
  od = __builtin_amdgcn_mfma_f32_32x32x16_bf16(pa1, PK(l1, h1), od, 0, 0, 0);
  od = __builtin_amdgcn_mfma_f32_32x32x16_bf16(pa2, PK(l2, h2), od, 0, 0, 0);
  od = __builtin_amdgcn_mfma_f32_32x32x16_bf16(pa3, PK(l3, h3), od, 0, 0, 0);
#undef PK
}
__device__ __forceinline__ void pv_d0(f32x16* o, int vb, bf16x8 pa0, bf16x8 pa1, bf16x8 pa2, bf16x8 pa3) {
  pv_one<0>(o[0], vb, pa0, pa1, pa2, pa3); pv_one<1>(o[1], vb, pa0, pa1, pa2, pa3); pv_one<2>(o[2], vb, pa0, pa1, pa2, pa3); pv_one<3>(o[3], vb, pa0, pa1, pa2, pa3);
}

__global__ __launch_bounds__(512, 1) void k_attn(const u16* __restrict__ Q, const u16* __restrict__ Kc, const u16* __restrict__ Vc, u16* __restrict__ O) {
  extern __shared__ __attribute__((aligned(16))) char lds[];
  const int b = blockIdx.x;
  const int head = (b & 7) + 8 * ((b >> 3) & 3), qb = b >> 5;
  const u16* Qb = Q + (long)qb * 256 * HD + head * DH;
  const u16* Kh = Kc + (long)head * S_ * DH;
  const u16* Vh = Vc + (long)head * S_ * DH;
  u16* Ob = O + (long)qb * 256 * HD + head * DH;
  const int tid = threadIdx.x, wid = tid >> 6, lane = tid & 63, r32 = lane & 31, hi = lane >> 5;
  u16* V_lds = (u16*)lds; u16* K_lds = (u16*)(lds + 2 * SHM_V);
  float* ws = (float*)(lds + 2 * SHM_V + 2 * SHM_K) + wid * 64; float* li_l = ws; float* al_l = ws + 32;
  float m_reg = -1e30f, l_reg = 0; f32x16 o[4] = {}; bf16x8 qr[8];
  const u16* Qw = Qb + (long)(wid * QBLK + r32) * HD + hi * 8;
#pragma unroll
  for (int d0 = 0; d0 < 8; ++d0) qr[d0] = *reinterpret_cast<const bf16x8*>(Qw + d0 * 16);
  const int sr = tid >> 4, sc = (tid & 15) * 8, vst0 = v_st(sr, sc), vst1 = v_st(32 + sr, sc);
  const int vb0 = (int)(uintptr_t)V_lds + v_rd_base(lane);
  struct { bf16x8 vs0, vs1, ks0, ks1; } sr_[2];
#define LD8(p) (*reinterpret_cast<const bf16x8*>(p))
#define SLOAD(i, k0) do { sr_[i].vs0 = LD8(&Vh[(long)((k0) + sr) * DH + sc]); sr_[i].vs1 = LD8(&Vh[(long)((k0) + 32 + sr) * DH + sc]); \
    sr_[i].ks0 = LD8(&Kh[(long)((k0) + sr) * DH + sc]); sr_[i].ks1 = LD8(&Kh[(long)((k0) + 32 + sr) * DH + sc]); } while (0)
#define SWRITE(b, i) do { *(bf16x8*)((char*)V_lds + (b) * SHM_V + vst0) = sr_[i].vs0;          \
    *(bf16x8*)((char*)V_lds + (b) * SHM_V + vst1) = sr_[i].vs1; int kc = sc * 2;               \
    *(bf16x8*)((char*)K_lds + (b) * SHM_K + KSWZ(sr, kc)) = sr_[i].ks0;                       \
    *(bf16x8*)((char*)K_lds + (b) * SHM_K + KSWZ(32 + sr, kc)) = sr_[i].ks1; } while (0)
#define SWAIT() asm volatile("s_waitcnt vmcnt(4)" ::: "memory")
#define RESC(a) do { if (__any((a) < 1.f)) { if (hi == 0) al_l[r32] = (a); asm volatile("s_waitcnt lgkmcnt(0)" ::: "memory"); \
    for (int d = 0; d < 4; ++d) for (int r = 0; r < 16; ++r) o[d][r] *= al_l[crow(r, hi)]; } } while (0)
  f32x16 pA0, pA1, pB0, pB1; float mnA, mnB, alA, alB; bf16x8 pa0, pa1, pa2, pa3; constexpr int NT = S_ / KVBLK;
  constexpr int SE = 0, SO = 1;
  SLOAD(SE, 0); asm volatile("s_waitcnt vmcnt(0)" ::: "memory"); SWRITE(0, SE); __syncthreads();
  qkt(pA0, pA1, K_lds, qr, r32, hi); partialSM(pA0, pA1, m_reg, mnA, alA);
  SLOAD(SO, KVBLK); SLOAD(SE, 2 * KVBLK);
  SWAIT(); SWRITE(1, SO); __syncthreads();
  for (int j = 1; j + 1 < NT; j += 2) {
    SBAR(); qkt(pB0, pB1, (u16*)((char*)K_lds + SHM_K), qr, r32, hi);
    finishSM(pA0, pA1, alA, l_reg, pa0, pa1, pa2, pa3); SBAR();
    SLOAD(SO, (j + 2) * KVBLK); SBAR();
    pv_d0(o, vb0, pa0, pa1, pa2, pa3); partialSM(pB0, pB1, m_reg, mnB, alB);
    __syncthreads(); SWAIT(); SWRITE(0, SE);
    RESC(alB); __syncthreads();
    SBAR(); qkt(pA0, pA1, K_lds, qr, r32, hi);
    finishSM(pB0, pB1, alB, l_reg, pa0, pa1, pa2, pa3); SBAR();
    if (j + 3 < NT) SLOAD(SE, (j + 3) * KVBLK); SBAR();
    pv_d0(o, vb0 + (int)SHM_V, pa0, pa1, pa2, pa3); partialSM(pA0, pA1, m_reg, mnA, alA);
    __syncthreads(); SWAIT(); SWRITE(1, SO);
    RESC(alA); __syncthreads();
  }
  SBAR(); qkt(pB0, pB1, (u16*)((char*)K_lds + SHM_K), qr, r32, hi);
  finishSM(pA0, pA1, alA, l_reg, pa0, pa1, pa2, pa3); SBAR();
  pv_d0(o, vb0, pa0, pa1, pa2, pa3); partialSM(pB0, pB1, m_reg, mnB, alB);
  __syncthreads(); RESC(alB);
  finishSM(pB0, pB1, alB, l_reg, pa0, pa1, pa2, pa3); SBAR();
  pv_d0(o, vb0 + (int)SHM_V, pa0, pa1, pa2, pa3);
  if (hi == 0) li_l[r32] = l_reg; asm volatile("s_waitcnt lgkmcnt(0)" ::: "memory");
  float rli[16];
#pragma unroll
  for (int r = 0; r < 16; ++r) rli[r] = __builtin_amdgcn_rcpf(li_l[crow(r, hi)]);
  u16* Ow = Ob + (long)(wid * QBLK) * HD;
#pragma unroll
  for (int r = 0; r < 16; ++r) { int orow = crow(r, hi);
#pragma unroll
    for (int d0 = 0; d0 < 4; ++d0) Ow[(long)orow * HD + d0 * 32 + r32] = f2bf(o[d0][r] * rli[r]); }
#undef SLOAD
#undef SWRITE
#undef SWAIT
#undef RESC
}

extern "C" void kernel_launch(void* const* d_in, const int* in_sizes, int n_in, void* d_out, int out_size, void* d_ws, size_t ws_size,
                              hipStream_t stream) {
  const u16* x = (const u16*)d_in[0];
  const u16* kc = (const u16*)d_in[1];
  const u16* vc = (const u16*)d_in[2];
  const u16* wq = (const u16*)d_in[3];
  const u16* wo = (const u16*)d_in[4];
  const u16* w_in = (const u16*)d_in[6];
  const u16* w_sp = (const u16*)d_in[7];
  const u16* b_sp = (const u16*)d_in[8];
  const u16* w_out = (const u16*)d_in[9];
  const u16* c = (const u16*)d_in[10];
  const u16* w_mod = (const u16*)d_in[11];
  u16* out = (u16*)d_out;

  char* ws = (char*)d_ws;
  size_t off = 0;
  auto carve = [&](size_t bytes) { void* p = ws + off; off += (bytes + 255) / 256 * 256; return p; };
  float* mod = (float*)carve(3072 * 4);
  u16* h1 = (u16*)carve((size_t)S_ * D_ * 2);
  u16* q = (u16*)carve((size_t)S_ * HD * 2);
  u16* o = (u16*)carve((size_t)S_ * HD * 2);
  float* part = (float*)carve((size_t)4 * S_ * D_ * 4);
  u16* x1 = (u16*)carve((size_t)S_ * D_ * 2);
  u16* h2 = (u16*)carve((size_t)S_ * D_ * 2);
  u16* z = (u16*)carve((size_t)S_ * F_ * 2);
  u16* z2n = (u16*)carve((size_t)S_ * FH * 2);
  u16* u = (u16*)carve((size_t)S_ * FH * 2);

  constexpr int GS = 2 * (128 * 64 * 2 + 64 * 128 * 2);
  hipFuncSetAttribute((const void*)k_attn, hipFuncAttributeMaxDynamicSharedMemorySize, (int)SHM_ATTN);

  k_mod<<<48, 256, 0, stream>>>(c, w_mod, mod);
  k_ln1<<<S_ / 4, 256, 0, stream>>>(x, mod, h1);
  { EpiArgs ea{q, nullptr, nullptr, nullptr, HD, 0};
    k_gemm<128, 128, 2, 2, EPI_BF16><<<dim3(HD / 128, S_ / 128, 1), 256, GS, stream>>>(h1, wq, ea, D_, HD, D_, D_); }
  k_attn<<<256, 512, SHM_ATTN, stream>>>(q, kc, vc, o);
  { EpiArgs ea{part, nullptr, nullptr, nullptr, D_, 0};
    k_gemm<128, 128, 2, 2, EPI_F32><<<dim3(D_ / 128, S_ / 128, 4), 256, GS, stream>>>(o, wo, ea, HD, D_, HD, HD / 4); }
  k_resid_ln2<4><<<S_ / 4, 256, 0, stream>>>(x, part, mod, x1, h2);
  { EpiArgs ea{z, nullptr, nullptr, nullptr, F_, 0};
    k_gemm<128, 128, 2, 2, EPI_GELU><<<dim3(F_ / 128, S_ / 128, 1), 256, GS, stream>>>(h2, w_in, ea, D_, F_, D_, D_); }
  k_ln_z2<<<S_ / 4, 256, 0, stream>>>(z, z2n);
  { EpiArgs ea{u, z, b_sp, nullptr, FH, 0};
    k_gemm<128, 128, 2, 2, EPI_SP><<<dim3(FH / 128, S_ / 128, 1), 256, GS, stream>>>(w_sp, z2n, ea, S_, FH, S_, S_); }
  { EpiArgs ea{out, x1, nullptr, mod + 5 * D_, D_, 0};
    k_gemm<128, 128, 2, 2, EPI_OUT><<<dim3(D_ / 128, S_ / 128, 1), 256, GS, stream>>>(u, w_out, ea, FH, D_, FH, FH); }
}
```

```cpp
#include <hip/hip_runtime.h>
#include <hip/hip_bf16.h>
#include <stdint.h>

typedef unsigned short u16;
typedef __attribute__((ext_vector_type(8))) short bf16x8;
typedef __attribute__((ext_vector_type(4))) short s16x4;
typedef __attribute__((ext_vector_type(16))) float f32x16;
typedef __attribute__((ext_vector_type(4))) unsigned u32x4;

constexpr int S_ = 2048, D_ = 512, H_ = 32, DH = 128, HD = H_ * DH;
constexpr int F_ = 1792, FH = 896;

__device__ __forceinline__ float bf2f(u16 v) { return __uint_as_float((unsigned)v << 16); }
__device__ __forceinline__ u16 f2bf(float f) {
  unsigned u = __float_as_uint(f);
  u += 0x7fffu + ((u >> 16) & 1u);
  return (u16)(u >> 16);
}
__device__ __forceinline__ float rbf(float f) { return bf2f(f2bf(f)); }
__device__ __forceinline__ unsigned cvtpk(float lo, float hi) {
  unsigned r; asm volatile("v_cvt_pk_bf16_f32 %0, %1, %2" : "=v"(r) : "v"(lo), "v"(hi)); return r;
}
__device__ __forceinline__ int crow(int r, int hi) { return (r & 3) + 8 * (r >> 2) + 4 * hi; }
typedef __attribute__((ext_vector_type(4))) __bf16 bfv4;
typedef __attribute__((address_space(3))) bfv4 lds_bfv4;
__device__ __forceinline__ s16x4 tr_read_b(const void* lds_ptr) {
  bfv4 v = __builtin_amdgcn_ds_read_tr16_b64_v4bf16((lds_bfv4*)(lds_ptr));
  return *reinterpret_cast<s16x4*>(&v);
}
__device__ __forceinline__ float wave_sum(float v) {
#pragma unroll
  for (int o = 32; o >= 1; o >>= 1) v += __shfl_xor(v, o, 64);
  return v;
}

__global__ __launch_bounds__(256) void k_mod(const u16* __restrict__ c, const u16* __restrict__ w_mod, float* __restrict__ mod) {
  __shared__ float red[32][65];
  const int cg = threadIdx.x & 7, kg = threadIdx.x >> 3;
  float acc[8];
#pragma unroll
  for (int j = 0; j < 8; ++j) acc[j] = 0.f;
  bf16x8 wv[16];
#pragma unroll
  for (int k = 0; k < 16; ++k) wv[k] = *reinterpret_cast<const bf16x8*>(w_mod + (long)(kg * 16 + k) * 3072 + blockIdx.x * 64 + cg * 8);
#pragma unroll
  for (int k = 0; k < 16; ++k) {
    float cv = bf2f(c[kg * 16 + k]);
    float sv = rbf(cv / (1.f + __expf(-cv)));
#pragma unroll
    for (int j = 0; j < 8; ++j) acc[j] += sv * bf2f((u16)wv[k][j]);
  }
#pragma unroll
  for (int j = 0; j < 8; ++j) red[kg][cg * 8 + j] = acc[j];
  __syncthreads();
  if (threadIdx.x < 64) {
    float t = 0.f;
#pragma unroll
    for (int g = 0; g < 32; ++g) t += red[g][threadIdx.x];
    mod[blockIdx.x * 64 + threadIdx.x] = rbf(t);
  }
}

__global__ __launch_bounds__(256) void k_ln1(const u16* __restrict__ x, const float* __restrict__ mod, u16* __restrict__ h) {
  const int lane = threadIdx.x & 63, row = blockIdx.x * 4 + (threadIdx.x >> 6);
  bf16x8 xv = *reinterpret_cast<const bf16x8*>(x + (long)row * D_ + lane * 8);
  float v[8]; float s = 0.f;
#pragma unroll
  for (int j = 0; j < 8; ++j) { v[j] = bf2f((u16)xv[j]); s += v[j]; }
  float mean = wave_sum(s) * (1.f / D_);
  float q = 0.f;
#pragma unroll
  for (int j = 0; j < 8; ++j) { float d = v[j] - mean; q += d * d; }
  float rstd = rsqrtf(wave_sum(q) * (1.f / D_) + 1e-5f);
  bf16x8 o;
#pragma unroll
  for (int j = 0; j < 8; ++j) {
    int cidx = lane * 8 + j;
    o[j] = (short)f2bf((v[j] - mean) * rstd * (1.f + mod[D_ + cidx]) + mod[cidx]);
  }
  *reinterpret_cast<bf16x8*>(h + (long)row * D_ + lane * 8) = o;
}

enum { EPI_BF16 = 0, EPI_F32 = 1, EPI_GELU = 2, EPI_SP = 3, EPI_OUT = 4 };
struct EpiArgs {
  void* C;
  const u16* aux0;
  const u16* aux1;
  const float* auxf;
  int ldc;
  int pad;
};

__device__ __forceinline__ float gelu_tanh(float x) {
  const float k0 = 0.7978845608028654f, k1 = 0.044715f;
  float u = k0 * (x + k1 * x * x * x);
  float e = __expf(2.f * u);
  float t = 1.f - 2.f / (e + 1.f);
  return 0.5f * x * (1.f + t);
}

template <int BM, int BN, int WM, int WN, int EPI>
__global__ __launch_bounds__(WM * WN * 64) void k_gemm(const u16* __restrict__ A, const u16* __restrict__ B, EpiArgs ea,
                                                      int lda, int ldb, int K, int ksplit) {
  constexpr int BK = 64, NT = WM * WN * 64;
  constexpr int TM = BM / WM / 32, TN = BN / WN / 32;
  constexpr int ACH = BM * 8 / NT, BCH = BN * 8 / NT;
  constexpr int ABYTES = BM * BK * 2, BBYTES = BK * BN * 2;
  extern __shared__ __attribute__((aligned(16))) char smem[];
  const int tid = threadIdx.x, lane = tid & 63, wid = tid >> 6;
  const int wm = wid / WN, wn = wid % WN;
  const int r32 = lane & 31, hi = lane >> 5;
  const int bm = blockIdx.y * BM, bn = blockIdx.x * BN;
  const int kbeg = blockIdx.z * ksplit;
  const int nt = ksplit / BK;
  A += (long)bm * lda + kbeg;
  B += (long)kbeg * ldb + bn;

  bf16x8 ra[ACH], rb[BCH];
  auto gload = [&](int t) {
#pragma unroll
    for (int i = 0; i < ACH; ++i) {
      int c = tid + i * NT; int row = c >> 3, ch = c & 7;
      ra[i] = *reinterpret_cast<const bf16x8*>(A + (long)row * lda + t * BK + ch * 8);
    }
#pragma unroll
    for (int i = 0; i < BCH; ++i) {
      int c = tid + i * NT; int row = c / (BN / 8), ch = c % (BN / 8);
      rb[i] = *reinterpret_cast<const bf16x8*>(B + (long)(t * BK + row) * ldb + ch * 8);
    }
  };
  auto swrite = [&](int buf) {
    char* sa = smem + buf * (ABYTES + BBYTES);
    char* sb = sa + ABYTES;
#pragma unroll
    for (int i = 0; i < ACH; ++i) {
      int c = tid + i * NT; int row = c >> 3, ch = c & 7;
      *reinterpret_cast<bf16x8*>(sa + row * 128 + ((ch ^ ((row >> 1) & 7)) << 4)) = ra[i];
    }
#pragma unroll
    for (int i = 0; i < BCH; ++i) {
      int c = tid + i * NT; int row = c / (BN / 8), ch = c % (BN / 8);
      *reinterpret_cast<bf16x8*>(sb + (ch >> 2) * 4096 + row * 64 + (ch & 3) * 16) = rb[i];
    }
  };
  f32x16 acc[TM][TN];
#pragma unroll
  for (int m = 0; m < TM; ++m)
#pragma unroll
    for (int n = 0; n < TN; ++n) acc[m][n] = f32x16{};

  gload(0); swrite(0); __syncthreads();
  const int q4 = (lane & 15) >> 2, p4 = lane & 3, blk = (lane >> 4) & 1;
  for (int t = 0; t < nt; ++t) {
    const int buf = t & 1;
    if (t + 1 < nt) gload(t + 1);
    const char* sa = smem + buf * (ABYTES + BBYTES);
    const char* sb = sa + ABYTES;
#pragma unroll
    for (int s = 0; s < 4; ++s) {
      bf16x8 af[TM], bfr[TN];
#pragma unroll
      for (int m = 0; m < TM; ++m) {
        int row = (wm * TM + m) * 32 + r32; int ch = 2 * s + hi;
        af[m] = *reinterpret_cast<const bf16x8*>(sa + row * 128 + ((ch ^ ((row >> 1) & 7)) << 4));
      }
#pragma unroll
      for (int n = 0; n < TN; ++n) {
        const char* pb = sb + (wn * TN + n) * 4096 + (16 * s + 8 * hi + q4) * 64 + blk * 32 + p4 * 8;
        s16x4 lo = tr_read_b(pb), hh = tr_read_b(pb + 256);
        bfr[n] = (bf16x8){lo[0], lo[1], lo[2], lo[3], hh[0], hh[1], hh[2], hh[3]};
      }
#pragma unroll
      for (int m = 0; m < TM; ++m)
#pragma unroll
        for (int n = 0; n < TN; ++n)
          acc[m][n] = __builtin_amdgcn_mfma_f32_32x32x16_bf16(af[m], bfr[n], acc[m][n], 0, 0, 0);
    }
    if (t + 1 < nt) swrite(buf ^ 1);
    __syncthreads();
  }
#pragma unroll
  for (int m = 0; m < TM; ++m)
#pragma unroll
    for (int n = 0; n < TN; ++n) {
      const int col = bn + (wn * TN + n) * 32 + r32;
#pragma unroll
      for (int r = 0; r < 16; ++r) {
        const int row = bm + (wm * TM + m) * 32 + crow(r, hi);
        float v = acc[m][n][r];
        if constexpr (EPI == EPI_BF16) {
          ((u16*)ea.C)[(long)row * ea.ldc + col] = f2bf(v);
        } else if constexpr (EPI == EPI_F32) {
          ((float*)ea.C)[((long)blockIdx.z * S_ + row) * ea.ldc + col] = v;
        } else if constexpr (EPI == EPI_GELU) {
          ((u16*)ea.C)[(long)row * ea.ldc + col] = f2bf(gelu_tanh(rbf(v)));
        } else if constexpr (EPI == EPI_SP) {
          float g = rbf(rbf(v) + bf2f(ea.aux1[row]));
          float z1 = bf2f(ea.aux0[(long)row * F_ + col]);
          ((u16*)ea.C)[(long)row * ea.ldc + col] = f2bf(z1 * g);
        } else {
          float x1 = bf2f(ea.aux0[(long)row * D_ + col]);
          ((u16*)ea.C)[(long)row * ea.ldc + col] = f2bf(x1 + rbf(ea.auxf[col] * rbf(v)));
        }
      }
    }
}

template <int NSPLIT>
__global__ __launch_bounds__(256) void k_resid_ln2(const u16* __restrict__ x, const float* __restrict__ part, const float* __restrict__ mod,
                                                   u16* __restrict__ x1o, u16* __restrict__ h2) {
  const int lane = threadIdx.x & 63, row = blockIdx.x * 4 + (threadIdx.x >> 6);
  bf16x8 xv = *reinterpret_cast<const bf16x8*>(x + (long)row * D_ + lane * 8);
  float a[8];
#pragma unroll
  for (int j = 0; j < 8; ++j) a[j] = 0.f;
#pragma unroll
  for (int p = 0; p < NSPLIT; ++p) {
    const float4* pp = reinterpret_cast<const float4*>(part + ((long)p * S_ + row) * D_ + lane * 8);
    float4 u0 = pp[0], u1 = pp[1];
    a[0] += u0.x; a[1] += u0.y; a[2] += u0.z; a[3] += u0.w; a[4] += u1.x; a[5] += u1.y; a[6] += u1.z; a[7] += u1.w;
  }
  float v[8]; float s = 0.f; bf16x8 xo;
#pragma unroll
  for (int j = 0; j < 8; ++j) {
    int cidx = lane * 8 + j;
    float t = rbf(bf2f((u16)xv[j]) + rbf(mod[2 * D_ + cidx] * rbf(a[j])));
    v[j] = t; s += t; xo[j] = (short)f2bf(t);
  }
  *reinterpret_cast<bf16x8*>(x1o + (long)row * D_ + lane * 8) = xo;
  float mean = wave_sum(s) * (1.f / D_);
  float q = 0.f;
#pragma unroll
  for (int j = 0; j < 8; ++j) { float d = v[j] - mean; q += d * d; }
  float rstd = rsqrtf(wave_sum(q) * (1.f / D_) + 1e-5f);
  bf16x8 o;
#pragma unroll
  for (int j = 0; j < 8; ++j) {
    int cidx = lane * 8 + j;
    o[j] = (short)f2bf((v[j] - mean) * rstd * (1.f + mod[4 * D_ + cidx]) + mod[3 * D_ + cidx]);
  }
  *reinterpret_cast<bf16x8*>(h2 + (long)row * D_ + lane * 8) = o;
}

__global__ __launch_bounds__(256) void k_ln_z2(const u16* __restrict__ z, u16* __restrict__ z2n) {
  const int lane = threadIdx.x & 63, row = blockIdx.x * 4 + (threadIdx.x >> 6);
  float v[16]; float s = 0.f; const bool act = lane < 56;
  bf16x8 c0 = {}, c1 = {};
  if (act) {
    c0 = *reinterpret_cast<const bf16x8*>(z + (long)row * F_ + FH + lane * 16);
    c1 = *reinterpret_cast<const bf16x8*>(z + (long)row * F_ + FH + lane * 16 + 8);
  }
#pragma unroll
  for (int j = 0; j < 8; ++j) { v[j] = bf2f((u16)c0[j]); v[8 + j] = bf2f((u16)c1[j]); s += v[j] + v[8 + j]; }
  float mean = wave_sum(s) * (1.f / FH);
  float q = 0.f;
  if (act) {
#pragma unroll
    for (int j = 0; j < 16; ++j) { float d = v[j] - mean; q += d * d; }
  }
  float rstd = rsqrtf(wave_sum(q) * (1.f / FH) + 1e-5f);
  if (act) {
    bf16x8 o0, o1;
#pragma unroll
    for (int j = 0; j < 8; ++j) { o0[j] = (short)f2bf((v[j] - mean) * rstd); o1[j] = (short)f2bf((v[8 + j] - mean) * rstd); }
    *reinterpret_cast<bf16x8*>(z2n + (long)row * FH + lane * 16) = o0;
    *reinterpret_cast<bf16x8*>(z2n + (long)row * FH + lane * 16 + 8) = o1;
  }
}

constexpr int NW = 8, QBLK = 32, KVBLK = 64;
constexpr float SCALE = 0.088388347648318440f;
constexpr float THR = 8.f;
constexpr size_t SHM_V = KVBLK * DH * 2, SHM_K = KVBLK * DH * 2, SHM_ATTN = 2 * SHM_V + 2 * SHM_K + NW * 64 * 4;
#define KSWZ(row, colB) ((row) * 256 + ((colB) ^ (((row) & 7) << 4)))
#define SBAR() __builtin_amdgcn_sched_barrier(0)

__device__ __forceinline__ void partialSM(f32x16& p0, f32x16& p1, float& m_reg, float& mn, float& alpha) {
  constexpr float C = SCALE * 1.4426950408889634f;
  float pmax = p0[0];
  for (int r = 1; r < 16; ++r) pmax = fmaxf(pmax, p0[r]);
  for (int r = 0; r < 16; ++r) pmax = fmaxf(pmax, p1[r]);
  { auto rr = __builtin_amdgcn_permlane32_swap(__float_as_uint(pmax), __float_as_uint(pmax), false, false);
    pmax = fmaxf(__uint_as_float(rr[0]), __uint_as_float(rr[1])); }
  if (__builtin_expect(__all(pmax - m_reg <= THR / SCALE), 1)) { mn = m_reg; alpha = 1.f; }
  else { mn = fmaxf(m_reg, pmax); alpha = __builtin_amdgcn_exp2f((m_reg - mn) * C); m_reg = mn; }
  float mnC = -mn * C;
  for (int r = 0; r < 16; ++r) p0[r] = fmaf(p0[r], C, mnC);
  for (int r = 0; r < 16; ++r) p1[r] = fmaf(p1[r], C, mnC);
  for (int r = 0; r < 16; ++r) p0[r] = __builtin_amdgcn_exp2f(p0[r]);
}
__device__ __forceinline__ void finishSM(f32x16& p0, f32x16& p1, float alpha, float& l_reg, bf16x8& pa0, bf16x8& pa1, bf16x8& pa2, bf16x8& pa3) {
  for (int r = 0; r < 16; ++r) p1[r] = __builtin_amdgcn_exp2f(p1[r]);
  float ps = 0;
  for (int r = 0; r < 16; ++r) ps += p0[r];
  for (int r = 0; r < 16; ++r) ps += p1[r];
  { auto rr = __builtin_amdgcn_permlane32_swap(__float_as_uint(ps), __float_as_uint(ps), false, false);
    ps = __uint_as_float(rr[0]) + __uint_as_float(rr[1]); }
  l_reg = l_reg * alpha + ps;
#define PK4(P, BASE, OUT) do { unsigned a0 = cvtpk(P[BASE + 0], P[BASE + 1]), a1 = cvtpk(P[BASE + 2], P[BASE + 3]);   \
    unsigned b0 = cvtpk(P[BASE + 4], P[BASE + 5]), b1 = cvtpk(P[BASE + 6], P[BASE + 7]);                              \
    auto r0 = __builtin_amdgcn_permlane32_swap(a0, b0, false, false); auto r1 = __builtin_amdgcn_permlane32_swap(a1, b1, false, false); \
    u32x4 w = {r0[0], r1[0], r0[1], r1[1]}; OUT = *reinterpret_cast<bf16x8*>(&w); } while (0)
  PK4(p0, 0, pa0); PK4(p0, 8, pa1); PK4(p1, 0, pa2); PK4(p1, 8, pa3);
#undef PK4
}
__device__ __forceinline__ void qkt(f32x16& p0, f32x16& p1, const u16* Ks, const bf16x8* qr, int r32, int hi) {
  p0 = f32x16{}; p1 = f32x16{};
  for (int d0 = 0; d0 < 8; ++d0) { int cb = (d0 * 16 + hi * 8) * 2;
    bf16x8 b0 = *reinterpret_cast<const bf16x8*>((const char*)Ks + KSWZ(r32, cb));
    bf16x8 b1 = *reinterpret_cast<const bf16x8*>((const char*)Ks + KSWZ(32 + r32, cb));
    p0 = __builtin_amdgcn_mfma_f32_32x32x16_bf16(b0, qr[d0], p0, 0, 0, 0);
    p1 = __builtin_amdgcn_mfma_f32_32x32x16_bf16(b1, qr[d0], p1, 0, 0, 0); }
}
__device__ __forceinline__ int v_st(int k, int c) { const int kk = (k & ~0xC) | ((k & 4) << 1) | ((k & 8) >> 1); return ((kk >> 3) * 4 + (c >> 5)) * 512 + ((kk & 7) * 32 + (c & 31)) * 2; }
__device__ __forceinline__ int v_rd_base(int lane) { return ((lane & 3) << 3) | (((lane >> 2) & 3) << 6) | (((lane >> 4) & 1) << 5) | (((lane >> 5) & 1) << 8); }
constexpr int v_rd_off(int d0, int ks, int half) { return d0 * 512 + ks * 4096 + half * 2048; }
template <int OFF> __device__ __forceinline__ s16x4 tr_read(int vb) {
  s16x4 r; asm volatile("ds_read_b64_tr_b16 %0, %1 offset:%2" : "=&v"(r) : "v"(vb), "i"(OFF) : "memory"); return r;
}
template <int D0> __device__ __forceinline__ void pv_one(f32x16& od, int vb, bf16x8 pa0, bf16x8 pa1, bf16x8 pa2, bf16x8 pa3) {
  const s16x4 l0 = tr_read<v_rd_off(D0, 0, 0)>(vb), h0 = tr_read<v_rd_off(D0, 0, 1)>(vb), l1 = tr_read<v_rd_off(D0, 1, 0)>(vb), h1 = tr_read<v_rd_off(D0, 1, 1)>(vb);
  const s16x4 l2 = tr_read<v_rd_off(D0, 2, 0)>(vb), h2 = tr_read<v_rd_off(D0, 2, 1)>(vb), l3 = tr_read<v_rd_off(D0, 3, 0)>(vb), h3 = tr_read<v_rd_off(D0, 3, 1)>(vb);
  asm volatile("s_waitcnt lgkmcnt(0)" ::: "memory"); SBAR();
#define PK(L, H) (bf16x8){L[0], L[1], L[2], L[3], H[0], H[1], H[2], H[3]}
  od = __builtin_amdgcn_mfma_f32_32x32x16_bf16(pa0, PK(l0, h0), od, 0, 0, 0);
  od = __builtin_amdgcn_mfma_f32_32x32x16_bf16(pa1, PK(l1, h1), od, 0, 0, 0);
  od = __builtin_amdgcn_mfma_f32_32x32x16_bf16(pa2, PK(l2, h2), od, 0, 0, 0);
  od = __builtin_amdgcn_mfma_f32_32x32x16_bf16(pa3, PK(l3, h3), od, 0, 0, 0);
#undef PK
}
__device__ __forceinline__ void pv_d0(f32x16* o, int vb, bf16x8 pa0, bf16x8 pa1, bf16x8 pa2, bf16x8 pa3) {
  pv_one<0>(o[0], vb, pa0, pa1, pa2, pa3); pv_one<1>(o[1], vb, pa0, pa1, pa2, pa3); pv_one<2>(o[2], vb, pa0, pa1, pa2, pa3); pv_one<3>(o[3], vb, pa0, pa1, pa2, pa3);
}

__global__ __launch_bounds__(512, 1) void k_attn(const u16* __restrict__ Q, const u16* __restrict__ Kc, const u16* __restrict__ Vc, u16* __restrict__ O) {
  extern __shared__ __attribute__((aligned(16))) char lds[];
  const int b = blockIdx.x;
  const int head = (b & 7) + 8 * ((b >> 3) & 3), qb = b >> 5;
  const u16* Qb = Q + (long)qb * 256 * HD + head * DH;
  const u16* Kh = Kc + (long)head * S_ * DH;
  const u16* Vh = Vc + (long)head * S_ * DH;
  u16* Ob = O + (long)qb * 256 * HD + head * DH;
  const int tid = threadIdx.x, wid = tid >> 6, lane = tid & 63, r32 = lane & 31, hi = lane >> 5;
  u16* V_lds = (u16*)lds; u16* K_lds = (u16*)(lds + 2 * SHM_V);
  float* ws = (float*)(lds + 2 * SHM_V + 2 * SHM_K) + wid * 64; float* li_l = ws; float* al_l = ws + 32;
  float m_reg = -1e30f, l_reg = 0; f32x16 o[4] = {}; bf16x8 qr[8];
  const u16* Qw = Qb + (long)(wid * QBLK + r32) * HD + hi * 8;
#pragma unroll
  for (int d0 = 0; d0 < 8; ++d0) qr[d0] = *reinterpret_cast<const bf16x8*>(Qw + d0 * 16);
  const int sr = tid >> 4, sc = (tid & 15) * 8, vst0 = v_st(sr, sc), vst1 = v_st(32 + sr, sc);
  const int vb0 = (int)(uintptr_t)V_lds + v_rd_base(lane);
  struct { bf16x8 vs0, vs1, ks0, ks1; } sr_[2];
#define LD8(p) (*reinterpret_cast<const bf16x8*>(p))
#define SLOAD(i, k0) do { sr_[i].vs0 = LD8(&Vh[(long)((k0) + sr) * DH + sc]); sr_[i].vs1 = LD8(&Vh[(long)((k0) + 32 + sr) * DH + sc]); \
    sr_[i].ks0 = LD8(&Kh[(long)((k0) + sr) * DH + sc]); sr_[i].ks1 = LD8(&Kh[(long)((k0) + 32 + sr) * DH + sc]); } while (0)
#define SWRITE(b, i) do { *(bf16x8*)((char*)V_lds + (b) * SHM_V + vst0) = sr_[i].vs0;          \
    *(bf16x8*)((char*)V_lds + (b) * SHM_V + vst1) = sr_[i].vs1; int kc = sc * 2;               \
    *(bf16x8*)((char*)K_lds + (b) * SHM_K + KSWZ(sr, kc)) = sr_[i].ks0;                       \
    *(bf16x8*)((char*)K_lds + (b) * SHM_K + KSWZ(32 + sr, kc)) = sr_[i].ks1; } while (0)
#define SWAIT() asm volatile("s_waitcnt vmcnt(4)" ::: "memory")
#define RESC(a) do { if (__any((a) < 1.f)) { if (hi == 0) al_l[r32] = (a); asm volatile("s_waitcnt lgkmcnt(0)" ::: "memory"); \
    for (int d = 0; d < 4; ++d) for (int r = 0; r < 16; ++r) o[d][r] *= al_l[crow(r, hi)]; } } while (0)
  f32x16 pA0, pA1, pB0, pB1; float mnA, mnB, alA, alB; bf16x8 pa0, pa1, pa2, pa3; constexpr int NT = S_ / KVBLK;
  constexpr int SE = 0, SO = 1;
  SLOAD(SE, 0); asm volatile("s_waitcnt vmcnt(0)" ::: "memory"); SWRITE(0, SE); __syncthreads();
  qkt(pA0, pA1, K_lds, qr, r32, hi); partialSM(pA0, pA1, m_reg, mnA, alA);
  SLOAD(SO, KVBLK); SLOAD(SE, 2 * KVBLK);
  SWAIT(); SWRITE(1, SO); __syncthreads();
  for (int j = 1; j + 1 < NT; j += 2) {
    SBAR(); qkt(pB0, pB1, (u16*)((char*)K_lds + SHM_K), qr, r32, hi);
    finishSM(pA0, pA1, alA, l_reg, pa0, pa1, pa2, pa3); SBAR();
    SLOAD(SO, (j + 2) * KVBLK); SBAR();
    pv_d0(o, vb0, pa0, pa1, pa2, pa3); partialSM(pB0, pB1, m_reg, mnB, alB);
    __syncthreads(); SWAIT(); SWRITE(0, SE);
    RESC(alB); __syncthreads();
    SBAR(); qkt(pA0, pA1, K_lds, qr, r32, hi);
    finishSM(pB0, pB1, alB, l_reg, pa0, pa1, pa2, pa3); SBAR();
    if (j + 3 < NT) SLOAD(SE, (j + 3) * KVBLK); SBAR();
    pv_d0(o, vb0 + (int)SHM_V, pa0, pa1, pa2, pa3); partialSM(pA0, pA1, m_reg, mnA, alA);
    __syncthreads(); SWAIT(); SWRITE(1, SO);
    RESC(alA); __syncthreads();
  }
  SBAR(); qkt(pB0, pB1, (u16*)((char*)K_lds + SHM_K), qr, r32, hi);
  finishSM(pA0, pA1, alA, l_reg, pa0, pa1, pa2, pa3); SBAR();
  pv_d0(o, vb0, pa0, pa1, pa2, pa3); partialSM(pB0, pB1, m_reg, mnB, alB);
  __syncthreads(); RESC(alB);
  finishSM(pB0, pB1, alB, l_reg, pa0, pa1, pa2, pa3); SBAR();
  pv_d0(o, vb0 + (int)SHM_V, pa0, pa1, pa2, pa3);
  if (hi == 0) li_l[r32] = l_reg; asm volatile("s_waitcnt lgkmcnt(0)" ::: "memory");
  float rli[16];
#pragma unroll
  for (int r = 0; r < 16; ++r) rli[r] = __builtin_amdgcn_rcpf(li_l[crow(r, hi)]);
  u16* Ow = Ob + (long)(wid * QBLK) * HD;
#pragma unroll
  for (int r = 0; r < 16; ++r) { int orow = crow(r, hi);
#pragma unroll
    for (int d0 = 0; d0 < 4; ++d0) Ow[(long)orow * HD + d0 * 32 + r32] = f2bf(o[d0][r] * rli[r]); }
#undef SLOAD
#undef SWRITE
#undef SWAIT
#undef RESC
}

extern "C" void kernel_launch(void* const* d_in, const int* in_sizes, int n_in, void* d_out, int out_size, void* d_ws, size_t ws_size,
                              hipStream_t stream) {
  const u16* x = (const u16*)d_in[0];
  const u16* kc = (const u16*)d_in[1];
  const u16* vc = (const u16*)d_in[2];
  const u16* wq = (const u16*)d_in[3];
  const u16* wo = (const u16*)d_in[4];
  const u16* w_in = (const u16*)d_in[6];
  const u16* w_sp = (const u16*)d_in[7];
  const u16* b_sp = (const u16*)d_in[8];
  const u16* w_out = (const u16*)d_in[9];
  const u16* c = (const u16*)d_in[10];
  const u16* w_mod = (const u16*)d_in[11];
  u16* out = (u16*)d_out;

  char* ws = (char*)d_ws;
  size_t off = 0;
  auto carve = [&](size_t bytes) { void* p = ws + off; off += (bytes + 255) / 256 * 256; return p; };
  float* mod = (float*)carve(3072 * 4);
  u16* h1 = (u16*)carve((size_t)S_ * D_ * 2);
  u16* q = (u16*)carve((size_t)S_ * HD * 2);
  u16* o = (u16*)carve((size_t)S_ * HD * 2);
  float* part = (float*)carve((size_t)4 * S_ * D_ * 4);
  u16* x1 = (u16*)carve((size_t)S_ * D_ * 2);
  u16* h2 = (u16*)carve((size_t)S_ * D_ * 2);
  u16* z = (u16*)carve((size_t)S_ * F_ * 2);
  u16* z2n = (u16*)carve((size_t)S_ * FH * 2);
  u16* u = (u16*)carve((size_t)S_ * FH * 2);

  constexpr int GS = 2 * (128 * 64 * 2 + 64 * 128 * 2);
  hipFuncSetAttribute((const void*)k_attn, hipFuncAttributeMaxDynamicSharedMemorySize, (int)SHM_ATTN);

  k_mod<<<48, 256, 0, stream>>>(c, w_mod, mod);
  k_ln1<<<S_ / 4, 256, 0, stream>>>(x, mod, h1);
  { EpiArgs ea{q, nullptr, nullptr, nullptr, HD, 0};
    k_gemm<128, 128, 2, 2, EPI_BF16><<<dim3(HD / 128, S_ / 128, 1), 256, GS, stream>>>(h1, wq, ea, D_, HD, D_, D_); }
  k_attn<<<256, 512, SHM_ATTN, stream>>>(q, kc, vc, o);
  { EpiArgs ea{part, nullptr, nullptr, nullptr, D_, 0};
    k_gemm<64, 128, 2, 2, EPI_F32><<<dim3(D_ / 128, S_ / 64, 4), 256, GS, stream>>>(o, wo, ea, HD, D_, HD, HD / 4); }
  k_resid_ln2<4><<<S_ / 4, 256, 0, stream>>>(x, part, mod, x1, h2);
  { EpiArgs ea{z, nullptr, nullptr, nullptr, F_, 0};
    k_gemm<64, 128, 2, 2, EPI_GELU><<<dim3(F_ / 128, S_ / 64, 1), 256, GS, stream>>>(h2, w_in, ea, D_, F_, D_, D_); }
  k_ln_z2<<<S_ / 4, 256, 0, stream>>>(z, z2n);
  { EpiArgs ea{u, z, b_sp, nullptr, FH, 0};
    k_gemm<64, 128, 2, 2, EPI_SP><<<dim3(FH / 128, S_ / 64, 1), 256, GS, stream>>>(w_sp, z2n, ea, S_, FH, S_, S_); }
  { EpiArgs ea{out, x1, nullptr, mod + 5 * D_, D_, 0};
    k_gemm<64, 64, 2, 2, EPI_OUT><<<dim3(D_ / 64, S_ / 64, 1), 256, GS, stream>>>(u, w_out, ea, FH, D_, FH, FH); }
}
```

```cpp
#include <hip/hip_runtime.h>
#include <hip/hip_bf16.h>
#include <stdint.h>

typedef unsigned short u16;
typedef __attribute__((ext_vector_type(8))) short bf16x8;
typedef __attribute__((ext_vector_type(4))) short s16x4;
typedef __attribute__((ext_vector_type(16))) float f32x16;
typedef __attribute__((ext_vector_type(4))) unsigned u32x4;

constexpr int S_ = 2048, D_ = 512, H_ = 32, DH = 128, HD = H_ * DH;
constexpr int F_ = 1792, FH = 896;

__device__ __forceinline__ float bf2f(u16 v) { return __uint_as_float((unsigned)v << 16); }
__device__ __forceinline__ u16 f2bf(float f) {
  unsigned u = __float_as_uint(f);
  u += 0x7fffu + ((u >> 16) & 1u);
  return (u16)(u >> 16);
}
__device__ __forceinline__ float rbf(float f) { return bf2f(f2bf(f)); }
__device__ __forceinline__ unsigned cvtpk(float lo, float hi) {
  unsigned r; asm volatile("v_cvt_pk_bf16_f32 %0, %1, %2" : "=v"(r) : "v"(lo), "v"(hi)); return r;
}
__device__ __forceinline__ int crow(int r, int hi) { return (r & 3) + 8 * (r >> 2) + 4 * hi; }
typedef __attribute__((ext_vector_type(4))) __bf16 bfv4;
typedef __attribute__((address_space(3))) bfv4 lds_bfv4;
__device__ __forceinline__ s16x4 tr_read_b(const void* lds_ptr) {
  bfv4 v = __builtin_amdgcn_ds_read_tr16_b64_v4bf16((lds_bfv4*)(lds_ptr));
  return *reinterpret_cast<s16x4*>(&v);
}
__device__ __forceinline__ float wave_sum(float v) {
#pragma unroll
  for (int o = 32; o >= 1; o >>= 1) v += __shfl_xor(v, o, 64);
  return v;
}

__global__ __launch_bounds__(256) void k_mod(const u16* __restrict__ c, const u16* __restrict__ w_mod, float* __restrict__ mod) {
  __shared__ float red[32][65];
  const int cg = threadIdx.x & 7, kg = threadIdx.x >> 3;
  float acc[8];
#pragma unroll
  for (int j = 0; j < 8; ++j) acc[j] = 0.f;
  bf16x8 wv[16];
#pragma unroll
  for (int k = 0; k < 16; ++k) wv[k] = *reinterpret_cast<const bf16x8*>(w_mod + (long)(kg * 16 + k) * 3072 + blockIdx.x * 64 + cg * 8);
#pragma unroll
  for (int k = 0; k < 16; ++k) {
    float cv = bf2f(c[kg * 16 + k]);
    float sv = rbf(cv / (1.f + __expf(-cv)));
#pragma unroll
    for (int j = 0; j < 8; ++j) acc[j] += sv * bf2f((u16)wv[k][j]);
  }
#pragma unroll
  for (int j = 0; j < 8; ++j) red[kg][cg * 8 + j] = acc[j];
  __syncthreads();
  if (threadIdx.x < 64) {
    float t = 0.f;
#pragma unroll
    for (int g = 0; g < 32; ++g) t += red[g][threadIdx.x];
    mod[blockIdx.x * 64 + threadIdx.x] = rbf(t);
  }
}

__global__ __launch_bounds__(256) void k_ln1(const u16* __restrict__ x, const float* __restrict__ mod, u16* __restrict__ h) {
  const int lane = threadIdx.x & 63, row = blockIdx.x * 4 + (threadIdx.x >> 6);
  bf16x8 xv = *reinterpret_cast<const bf16x8*>(x + (long)row * D_ + lane * 8);
  float v[8]; float s = 0.f;
#pragma unroll
  for (int j = 0; j < 8; ++j) { v[j] = bf2f((u16)xv[j]); s += v[j]; }
  float mean = wave_sum(s) * (1.f / D_);
  float q = 0.f;
#pragma unroll
  for (int j = 0; j < 8; ++j) { float d = v[j] - mean; q += d * d; }
  float rstd = rsqrtf(wave_sum(q) * (1.f / D_) + 1e-5f);
  bf16x8 o;
#pragma unroll
  for (int j = 0; j < 8; ++j) {
    int cidx = lane * 8 + j;
    o[j] = (short)f2bf((v[j] - mean) * rstd * (1.f + mod[D_ + cidx]) + mod[cidx]);
  }
  *reinterpret_cast<bf16x8*>(h + (long)row * D_ + lane * 8) = o;
}

enum { EPI_BF16 = 0, EPI_F32 = 1, EPI_GELU = 2, EPI_SP = 3, EPI_OUT = 4 };
struct EpiArgs {
  void* C;
  const u16* aux0;
  const u16* aux1;
  const float* auxf;
  int ldc;
  int pad;
};

__device__ __forceinline__ float gelu_tanh(float x) {
  const float k0 = 0.7978845608028654f, k1 = 0.044715f;
  float u = k0 * (x + k1 * x * x * x);
  float e = __expf(2.f * u);
  float t = 1.f - 2.f / (e + 1.f);
  return 0.5f * x * (1.f + t);
}

template <int N> __device__ __forceinline__ void wait_vm() { asm volatile("s_waitcnt vmcnt(%0)" :: "n"(N) : "memory"); }
typedef __attribute__((address_space(3))) void lds_void;
typedef __attribute__((address_space(1))) const void glb_cvoid;
__device__ __forceinline__ void glds16(const void* g, void* l) {
  __builtin_amdgcn_global_load_lds((glb_cvoid*)g, (lds_void*)l, 16, 0, 0);
}
template <int BM, int BN, int WM, int WN, int EPI, int NST>
__global__ __launch_bounds__(WM * WN * 64) void k_gemm(const u16* __restrict__ A, const u16* __restrict__ B, EpiArgs ea,
                                                      int lda, int ldb, int K, int ksplit) {
  constexpr int BK = 64, NT = WM * WN * 64;
  constexpr int TM = BM / WM / 32, TN = BN / WN / 32;
  constexpr int ACH = BM * 8 / NT, BCH = BN * 8 / NT, G = ACH + BCH;
  constexpr int ABYTES = BM * BK * 2, BBYTES = BK * BN * 2, STB = ABYTES + BBYTES;
  extern __shared__ __attribute__((aligned(16))) char smem[];
  const int tid = threadIdx.x, lane = tid & 63, wid = tid >> 6;
  const int wm = wid / WN, wn = wid % WN;
  const int r32 = lane & 31, hi = lane >> 5;
  const int bm = blockIdx.y * BM, bn = blockIdx.x * BN;
  const int kbeg = blockIdx.z * ksplit;
  const int nt = ksplit / BK;
  A += (long)bm * lda + kbeg;
  B += (long)kbeg * ldb + bn;
  auto stage = [&](int t, int buf) {
    char* sa = smem + buf * STB;
    char* sb = sa + ABYTES;
#pragma unroll
    for (int i = 0; i < ACH; ++i) {
      int c = tid + i * NT; int row = c >> 3, ch = (c & 7) ^ ((row >> 1) & 7);
      glds16(A + (long)row * lda + t * BK + ch * 8, sa + c * 16);
    }
#pragma unroll
    for (int i = 0; i < BCH; ++i) {
      int c = tid + i * NT; int panel = c >> 8, row = (c & 255) >> 2, ch = c & 3;
      glds16(B + (long)(t * BK + row) * ldb + panel * 32 + ch * 8, sb + c * 16);
    }
  };
  f32x16 acc[TM][TN];
#pragma unroll
  for (int m = 0; m < TM; ++m)
#pragma unroll
    for (int n = 0; n < TN; ++n) acc[m][n] = f32x16{};
#pragma unroll
  for (int s0 = 0; s0 < NST - 1; ++s0) stage(s0, s0);
  const int q4 = (lane & 15) >> 2, p4 = lane & 3, blk = (lane >> 4) & 1;
  int buf = 0, nbuf = NST - 1;
  for (int t = 0; t < nt; ++t) {
    if (t + NST - 2 < nt) wait_vm<(NST - 2) * G>(); else wait_vm<0>();
    __builtin_amdgcn_s_barrier();
    if (t + NST - 1 < nt) stage(t + NST - 1, nbuf);
    const char* sa = smem + buf * STB;
    const char* sb = sa + ABYTES;
#pragma unroll
    for (int s = 0; s < 4; ++s) {
      bf16x8 af[TM], bfr[TN];
#pragma unroll
      for (int m = 0; m < TM; ++m) {
        int row = (wm * TM + m) * 32 + r32; int ch = 2 * s + hi;
        af[m] = *reinterpret_cast<const bf16x8*>(sa + row * 128 + ((ch ^ ((row >> 1) & 7)) << 4));
      }
#pragma unroll
      for (int n = 0; n < TN; ++n) {
        const char* pb = sb + (wn * TN + n) * 4096 + (16 * s + 8 * hi + q4) * 64 + blk * 32 + p4 * 8;
        s16x4 lo = tr_read_b(pb), hh = tr_read_b(pb + 256);
        bfr[n] = (bf16x8){lo[0], lo[1], lo[2], lo[3], hh[0], hh[1], hh[2], hh[3]};
      }
#pragma unroll
      for (int m = 0; m < TM; ++m)
#pragma unroll
        for (int n = 0; n < TN; ++n)
          acc[m][n] = __builtin_amdgcn_mfma_f32_32x32x16_bf16(af[m], bfr[n], acc[m][n], 0, 0, 0);
    }
    asm volatile("s_waitcnt lgkmcnt(0)" ::: "memory");
    buf = (buf + 1 == NST) ? 0 : buf + 1;
    nbuf = (nbuf + 1 == NST) ? 0 : nbuf + 1;
  }
#pragma unroll
  for (int m = 0; m < TM; ++m)
#pragma unroll
    for (int n = 0; n < TN; ++n) {
      const int col = bn + (wn * TN + n) * 32 + r32;
#pragma unroll
      for (int r = 0; r < 16; ++r) {
        const int row = bm + (wm * TM + m) * 32 + crow(r, hi);
        float v = acc[m][n][r];
        if constexpr (EPI == EPI_BF16) {
          ((u16*)ea.C)[(long)row * ea.ldc + col] = f2bf(v);
        } else if constexpr (EPI == EPI_F32) {
          ((float*)ea.C)[((long)blockIdx.z * S_ + row) * ea.ldc + col] = v;
        } else if constexpr (EPI == EPI_GELU) {
          ((u16*)ea.C)[(long)row * ea.ldc + col] = f2bf(gelu_tanh(rbf(v)));
        } else if constexpr (EPI == EPI_SP) {
          float g = rbf(rbf(v) + bf2f(ea.aux1[row]));
          float z1 = bf2f(ea.aux0[(long)row * F_ + col]);
          ((u16*)ea.C)[(long)row * ea.ldc + col] = f2bf(z1 * g);
        } else {
          float x1 = bf2f(ea.aux0[(long)row * D_ + col]);
          ((u16*)ea.C)[(long)row * ea.ldc + col] = f2bf(x1 + rbf(ea.auxf[col] * rbf(v)));
        }
      }
    }
}

template <int NSPLIT>
__global__ __launch_bounds__(256) void k_resid_ln2(const u16* __restrict__ x, const float* __restrict__ part, const float* __restrict__ mod,
                                                   u16* __restrict__ x1o, u16* __restrict__ h2) {
  const int lane = threadIdx.x & 63, row = blockIdx.x * 4 + (threadIdx.x >> 6);
  bf16x8 xv = *reinterpret_cast<const bf16x8*>(x + (long)row * D_ + lane * 8);
  float a[8];
#pragma unroll
  for (int j = 0; j < 8; ++j) a[j] = 0.f;
#pragma unroll
  for (int p = 0; p < NSPLIT; ++p) {
    const float4* pp = reinterpret_cast<const float4*>(part + ((long)p * S_ + row) * D_ + lane * 8);
    float4 u0 = pp[0], u1 = pp[1];
    a[0] += u0.x; a[1] += u0.y; a[2] += u0.z; a[3] += u0.w; a[4] += u1.x; a[5] += u1.y; a[6] += u1.z; a[7] += u1.w;
  }
  float v[8]; float s = 0.f; bf16x8 xo;
#pragma unroll
  for (int j = 0; j < 8; ++j) {
    int cidx = lane * 8 + j;
    float t = rbf(bf2f((u16)xv[j]) + rbf(mod[2 * D_ + cidx] * rbf(a[j])));
    v[j] = t; s += t; xo[j] = (short)f2bf(t);
  }
  *reinterpret_cast<bf16x8*>(x1o + (long)row * D_ + lane * 8) = xo;
  float mean = wave_sum(s) * (1.f / D_);
  float q = 0.f;
#pragma unroll
  for (int j = 0; j < 8; ++j) { float d = v[j] - mean; q += d * d; }
  float rstd = rsqrtf(wave_sum(q) * (1.f / D_) + 1e-5f);
  bf16x8 o;
#pragma unroll
  for (int j = 0; j < 8; ++j) {
    int cidx = lane * 8 + j;
    o[j] = (short)f2bf((v[j] - mean) * rstd * (1.f + mod[4 * D_ + cidx]) + mod[3 * D_ + cidx]);
  }
  *reinterpret_cast<bf16x8*>(h2 + (long)row * D_ + lane * 8) = o;
}

__global__ __launch_bounds__(256) void k_ln_z2(const u16* __restrict__ z, u16* __restrict__ z2n) {
  const int lane = threadIdx.x & 63, row = blockIdx.x * 4 + (threadIdx.x >> 6);
  float v[16]; float s = 0.f; const bool act = lane < 56;
  bf16x8 c0 = {}, c1 = {};
  if (act) {
    c0 = *reinterpret_cast<const bf16x8*>(z + (long)row * F_ + FH + lane * 16);
    c1 = *reinterpret_cast<const bf16x8*>(z + (long)row * F_ + FH + lane * 16 + 8);
  }
#pragma unroll
  for (int j = 0; j < 8; ++j) { v[j] = bf2f((u16)c0[j]); v[8 + j] = bf2f((u16)c1[j]); s += v[j] + v[8 + j]; }
  float mean = wave_sum(s) * (1.f / FH);
  float q = 0.f;
  if (act) {
#pragma unroll
    for (int j = 0; j < 16; ++j) { float d = v[j] - mean; q += d * d; }
  }
  float rstd = rsqrtf(wave_sum(q) * (1.f / FH) + 1e-5f);
  if (act) {
    bf16x8 o0, o1;
#pragma unroll
    for (int j = 0; j < 8; ++j) { o0[j] = (short)f2bf((v[j] - mean) * rstd); o1[j] = (short)f2bf((v[8 + j] - mean) * rstd); }
    *reinterpret_cast<bf16x8*>(z2n + (long)row * FH + lane * 16) = o0;
    *reinterpret_cast<bf16x8*>(z2n + (long)row * FH + lane * 16 + 8) = o1;
  }
}

constexpr int NW = 8, QBLK = 32, KVBLK = 64;
constexpr float SCALE = 0.088388347648318440f;
constexpr float THR = 8.f;
constexpr size_t SHM_V = KVBLK * DH * 2, SHM_K = KVBLK * DH * 2, SHM_ATTN = 2 * SHM_V + 2 * SHM_K + NW * 64 * 4;
#define KSWZ(row, colB) ((row) * 256 + ((colB) ^ (((row) & 7) << 4)))
#define SBAR() __builtin_amdgcn_sched_barrier(0)

__device__ __forceinline__ void partialSM(f32x16& p0, f32x16& p1, float& m_reg, float& mn, float& alpha) {
  constexpr float C = SCALE * 1.4426950408889634f;
  float pmax = p0[0];
  for (int r = 1; r < 16; ++r) pmax = fmaxf(pmax, p0[r]);
  for (int r = 0; r < 16; ++r) pmax = fmaxf(pmax, p1[r]);
  { auto rr = __builtin_amdgcn_permlane32_swap(__float_as_uint(pmax), __float_as_uint(pmax), false, false);
    pmax = fmaxf(__uint_as_float(rr[0]), __uint_as_float(rr[1])); }
  if (__builtin_expect(__all(pmax - m_reg <= THR / SCALE), 1)) { mn = m_reg; alpha = 1.f; }
  else { mn = fmaxf(m_reg, pmax); alpha = __builtin_amdgcn_exp2f((m_reg - mn) * C); m_reg = mn; }
  float mnC = -mn * C;
  for (int r = 0; r < 16; ++r) p0[r] = fmaf(p0[r], C, mnC);
  for (int r = 0; r < 16; ++r) p1[r] = fmaf(p1[r], C, mnC);
  for (int r = 0; r < 16; ++r) p0[r] = __builtin_amdgcn_exp2f(p0[r]);
}
__device__ __forceinline__ void finishSM(f32x16& p0, f32x16& p1, float alpha, float& l_reg, bf16x8& pa0, bf16x8& pa1, bf16x8& pa2, bf16x8& pa3) {
  for (int r = 0; r < 16; ++r) p1[r] = __builtin_amdgcn_exp2f(p1[r]);
  float ps = 0;
  for (int r = 0; r < 16; ++r) ps += p0[r];
  for (int r = 0; r < 16; ++r) ps += p1[r];
  { auto rr = __builtin_amdgcn_permlane32_swap(__float_as_uint(ps), __float_as_uint(ps), false, false);
    ps = __uint_as_float(rr[0]) + __uint_as_float(rr[1]); }
  l_reg = l_reg * alpha + ps;
#define PK4(P, BASE, OUT) do { unsigned a0 = cvtpk(P[BASE + 0], P[BASE + 1]), a1 = cvtpk(P[BASE + 2], P[BASE + 3]);   \
    unsigned b0 = cvtpk(P[BASE + 4], P[BASE + 5]), b1 = cvtpk(P[BASE + 6], P[BASE + 7]);                              \
    auto r0 = __builtin_amdgcn_permlane32_swap(a0, b0, false, false); auto r1 = __builtin_amdgcn_permlane32_swap(a1, b1, false, false); \
    u32x4 w = {r0[0], r1[0], r0[1], r1[1]}; OUT = *reinterpret_cast<bf16x8*>(&w); } while (0)
  PK4(p0, 0, pa0); PK4(p0, 8, pa1); PK4(p1, 0, pa2); PK4(p1, 8, pa3);
#undef PK4
}
__device__ __forceinline__ void qkt(f32x16& p0, f32x16& p1, const u16* Ks, const bf16x8* qr, int r32, int hi) {
  p0 = f32x16{}; p1 = f32x16{};
  for (int d0 = 0; d0 < 8; ++d0) { int cb = (d0 * 16 + hi * 8) * 2;
    bf16x8 b0 = *reinterpret_cast<const bf16x8*>((const char*)Ks + KSWZ(r32, cb));
    bf16x8 b1 = *reinterpret_cast<const bf16x8*>((const char*)Ks + KSWZ(32 + r32, cb));
    p0 = __builtin_amdgcn_mfma_f32_32x32x16_bf16(b0, qr[d0], p0, 0, 0, 0);
    p1 = __builtin_amdgcn_mfma_f32_32x32x16_bf16(b1, qr[d0], p1, 0, 0, 0); }
}
__device__ __forceinline__ int v_st(int k, int c) { const int kk = (k & ~0xC) | ((k & 4) << 1) | ((k & 8) >> 1); return ((kk >> 3) * 4 + (c >> 5)) * 512 + ((kk & 7) * 32 + (c & 31)) * 2; }
__device__ __forceinline__ int v_rd_base(int lane) { return ((lane & 3) << 3) | (((lane >> 2) & 3) << 6) | (((lane >> 4) & 1) << 5) | (((lane >> 5) & 1) << 8); }
constexpr int v_rd_off(int d0, int ks, int half) { return d0 * 512 + ks * 4096 + half * 2048; }
template <int OFF> __device__ __forceinline__ s16x4 tr_read(int vb) {
  s16x4 r; asm volatile("ds_read_b64_tr_b16 %0, %1 offset:%2" : "=&v"(r) : "v"(vb), "i"(OFF) : "memory"); return r;
}
template <int D0> __device__ __forceinline__ void pv_one(f32x16& od, int vb, bf16x8 pa0, bf16x8 pa1, bf16x8 pa2, bf16x8 pa3) {
  const s16x4 l0 = tr_read<v_rd_off(D0, 0, 0)>(vb), h0 = tr_read<v_rd_off(D0, 0, 1)>(vb), l1 = tr_read<v_rd_off(D0, 1, 0)>(vb), h1 = tr_read<v_rd_off(D0, 1, 1)>(vb);
  const s16x4 l2 = tr_read<v_rd_off(D0, 2, 0)>(vb), h2 = tr_read<v_rd_off(D0, 2, 1)>(vb), l3 = tr_read<v_rd_off(D0, 3, 0)>(vb), h3 = tr_read<v_rd_off(D0, 3, 1)>(vb);
  asm volatile("s_waitcnt lgkmcnt(0)" ::: "memory"); SBAR();
#define PK(L, H) (bf16x8){L[0], L[1], L[2], L[3], H[0], H[1], H[2], H[3]}
  od = __builtin_amdgcn_mfma_f32_32x32x16_bf16(pa0, PK(l0, h0), od, 0, 0, 0);
  od = __builtin_amdgcn_mfma_f32_32x32x16_bf16(pa1, PK(l1, h1), od, 0, 0, 0);
  od = __builtin_amdgcn_mfma_f32_32x32x16_bf16(pa2, PK(l2, h2), od, 0, 0, 0);
  od = __builtin_amdgcn_mfma_f32_32x32x16_bf16(pa3, PK(l3, h3), od, 0, 0, 0);
#undef PK
}
__device__ __forceinline__ void pv_d0(f32x16* o, int vb, bf16x8 pa0, bf16x8 pa1, bf16x8 pa2, bf16x8 pa3) {
  pv_one<0>(o[0], vb, pa0, pa1, pa2, pa3); pv_one<1>(o[1], vb, pa0, pa1, pa2, pa3); pv_one<2>(o[2], vb, pa0, pa1, pa2, pa3); pv_one<3>(o[3], vb, pa0, pa1, pa2, pa3);
}

__global__ __launch_bounds__(512, 1) void k_attn(const u16* __restrict__ Q, const u16* __restrict__ Kc, const u16* __restrict__ Vc, u16* __restrict__ O) {
  extern __shared__ __attribute__((aligned(16))) char lds[];
  const int b = blockIdx.x;
  const int head = (b & 7) + 8 * ((b >> 3) & 3), qb = b >> 5;
  const u16* Qb = Q + (long)qb * 256 * HD + head * DH;
  const u16* Kh = Kc + (long)head * S_ * DH;
  const u16* Vh = Vc + (long)head * S_ * DH;
  u16* Ob = O + (long)qb * 256 * HD + head * DH;
  const int tid = threadIdx.x, wid = tid >> 6, lane = tid & 63, r32 = lane & 31, hi = lane >> 5;
  u16* V_lds = (u16*)lds; u16* K_lds = (u16*)(lds + 2 * SHM_V);
  float* ws = (float*)(lds + 2 * SHM_V + 2 * SHM_K) + wid * 64; float* li_l = ws; float* al_l = ws + 32;
  float m_reg = -1e30f, l_reg = 0; f32x16 o[4] = {}; bf16x8 qr[8];
  const u16* Qw = Qb + (long)(wid * QBLK + r32) * HD + hi * 8;
#pragma unroll
  for (int d0 = 0; d0 < 8; ++d0) qr[d0] = *reinterpret_cast<const bf16x8*>(Qw + d0 * 16);
  const int sr = tid >> 4, sc = (tid & 15) * 8, vst0 = v_st(sr, sc), vst1 = v_st(32 + sr, sc);
  const int vb0 = (int)(uintptr_t)V_lds + v_rd_base(lane);
  struct { bf16x8 vs0, vs1, ks0, ks1; } sr_[2];
#define LD8(p) (*reinterpret_cast<const bf16x8*>(p))
#define SLOAD(i, k0) do { sr_[i].vs0 = LD8(&Vh[(long)((k0) + sr) * DH + sc]); sr_[i].vs1 = LD8(&Vh[(long)((k0) + 32 + sr) * DH + sc]); \
    sr_[i].ks0 = LD8(&Kh[(long)((k0) + sr) * DH + sc]); sr_[i].ks1 = LD8(&Kh[(long)((k0) + 32 + sr) * DH + sc]); } while (0)
#define SWRITE(b, i) do { *(bf16x8*)((char*)V_lds + (b) * SHM_V + vst0) = sr_[i].vs0;          \
    *(bf16x8*)((char*)V_lds + (b) * SHM_V + vst1) = sr_[i].vs1; int kc = sc * 2;               \
    *(bf16x8*)((char*)K_lds + (b) * SHM_K + KSWZ(sr, kc)) = sr_[i].ks0;                       \
    *(bf16x8*)((char*)K_lds + (b) * SHM_K + KSWZ(32 + sr, kc)) = sr_[i].ks1; } while (0)
#define SWAIT() asm volatile("s_waitcnt vmcnt(4)" ::: "memory")
#define RESC(a) do { if (__any((a) < 1.f)) { if (hi == 0) al_l[r32] = (a); asm volatile("s_waitcnt lgkmcnt(0)" ::: "memory"); \
    for (int d = 0; d < 4; ++d) for (int r = 0; r < 16; ++r) o[d][r] *= al_l[crow(r, hi)]; } } while (0)
  f32x16 pA0, pA1, pB0, pB1; float mnA, mnB, alA, alB; bf16x8 pa0, pa1, pa2, pa3; constexpr int NT = S_ / KVBLK;
  constexpr int SE = 0, SO = 1;
  SLOAD(SE, 0); asm volatile("s_waitcnt vmcnt(0)" ::: "memory"); SWRITE(0, SE); __syncthreads();
  qkt(pA0, pA1, K_lds, qr, r32, hi); partialSM(pA0, pA1, m_reg, mnA, alA);
  SLOAD(SO, KVBLK); SLOAD(SE, 2 * KVBLK);
  SWAIT(); SWRITE(1, SO); __syncthreads();
  for (int j = 1; j + 1 < NT; j += 2) {
    SBAR(); qkt(pB0, pB1, (u16*)((char*)K_lds + SHM_K), qr, r32, hi);
    finishSM(pA0, pA1, alA, l_reg, pa0, pa1, pa2, pa3); SBAR();
    SLOAD(SO, (j + 2) * KVBLK); SBAR();
    pv_d0(o, vb0, pa0, pa1, pa2, pa3); partialSM(pB0, pB1, m_reg, mnB, alB);
    __syncthreads(); SWAIT(); SWRITE(0, SE);
    RESC(alB); __syncthreads();
    SBAR(); qkt(pA0, pA1, K_lds, qr, r32, hi);
    finishSM(pB0, pB1, alB, l_reg, pa0, pa1, pa2, pa3); SBAR();
    if (j + 3 < NT) SLOAD(SE, (j + 3) * KVBLK); SBAR();
    pv_d0(o, vb0 + (int)SHM_V, pa0, pa1, pa2, pa3); partialSM(pA0, pA1, m_reg, mnA, alA);
    __syncthreads(); SWAIT(); SWRITE(1, SO);
    RESC(alA); __syncthreads();
  }
  SBAR(); qkt(pB0, pB1, (u16*)((char*)K_lds + SHM_K), qr, r32, hi);
  finishSM(pA0, pA1, alA, l_reg, pa0, pa1, pa2, pa3); SBAR();
  pv_d0(o, vb0, pa0, pa1, pa2, pa3); partialSM(pB0, pB1, m_reg, mnB, alB);
  __syncthreads(); RESC(alB);
  finishSM(pB0, pB1, alB, l_reg, pa0, pa1, pa2, pa3); SBAR();
  pv_d0(o, vb0 + (int)SHM_V, pa0, pa1, pa2, pa3);
  if (hi == 0) li_l[r32] = l_reg; asm volatile("s_waitcnt lgkmcnt(0)" ::: "memory");
  float rli[16];
#pragma unroll
  for (int r = 0; r < 16; ++r) rli[r] = __builtin_amdgcn_rcpf(li_l[crow(r, hi)]);
  u16* Ow = Ob + (long)(wid * QBLK) * HD;
#pragma unroll
  for (int r = 0; r < 16; ++r) { int orow = crow(r, hi);
#pragma unroll
    for (int d0 = 0; d0 < 4; ++d0) Ow[(long)orow * HD + d0 * 32 + r32] = f2bf(o[d0][r] * rli[r]); }
#undef SLOAD
#undef SWRITE
#undef SWAIT
#undef RESC
}

extern "C" void kernel_launch(void* const* d_in, const int* in_sizes, int n_in, void* d_out, int out_size, void* d_ws, size_t ws_size,
                              hipStream_t stream) {
  const u16* x = (const u16*)d_in[0];
  const u16* kc = (const u16*)d_in[1];
  const u16* vc = (const u16*)d_in[2];
  const u16* wq = (const u16*)d_in[3];
  const u16* wo = (const u16*)d_in[4];
  const u16* w_in = (const u16*)d_in[6];
  const u16* w_sp = (const u16*)d_in[7];
  const u16* b_sp = (const u16*)d_in[8];
  const u16* w_out = (const u16*)d_in[9];
  const u16* c = (const u16*)d_in[10];
  const u16* w_mod = (const u16*)d_in[11];
  u16* out = (u16*)d_out;

  char* ws = (char*)d_ws;
  size_t off = 0;
  auto carve = [&](size_t bytes) { void* p = ws + off; off += (bytes + 255) / 256 * 256; return p; };
  float* mod = (float*)carve(3072 * 4);
  u16* h1 = (u16*)carve((size_t)S_ * D_ * 2);
  u16* q = (u16*)carve((size_t)S_ * HD * 2);
  u16* o = (u16*)carve((size_t)S_ * HD * 2);
  float* part = (float*)carve((size_t)4 * S_ * D_ * 4);
  u16* x1 = (u16*)carve((size_t)S_ * D_ * 2);
  u16* h2 = (u16*)carve((size_t)S_ * D_ * 2);
  u16* z = (u16*)carve((size_t)S_ * F_ * 2);
  u16* z2n = (u16*)carve((size_t)S_ * FH * 2);
  u16* u = (u16*)carve((size_t)S_ * FH * 2);

  constexpr int GS = 2 * (128 * 64 * 2 + 64 * 128 * 2);
  hipFuncSetAttribute((const void*)k_gemm<64, 128, 2, 2, EPI_BF16, 3>, hipFuncAttributeMaxDynamicSharedMemorySize, 3 * 24576);
  hipFuncSetAttribute((const void*)k_gemm<64, 128, 2, 2, EPI_F32, 3>, hipFuncAttributeMaxDynamicSharedMemorySize, 3 * 24576);
  hipFuncSetAttribute((const void*)k_gemm<64, 128, 2, 2, EPI_GELU, 3>, hipFuncAttributeMaxDynamicSharedMemorySize, 3 * 24576);
  hipFuncSetAttribute((const void*)k_gemm<64, 128, 2, 2, EPI_SP, 4>, hipFuncAttributeMaxDynamicSharedMemorySize, 4 * 24576);
  hipFuncSetAttribute((const void*)k_gemm<64, 64, 2, 2, EPI_OUT, 4>, hipFuncAttributeMaxDynamicSharedMemorySize, 4 * 16384);
  hipFuncSetAttribute((const void*)k_attn, hipFuncAttributeMaxDynamicSharedMemorySize, (int)SHM_ATTN);

  k_mod<<<48, 256, 0, stream>>>(c, w_mod, mod);
  k_ln1<<<S_ / 4, 256, 0, stream>>>(x, mod, h1);
  { EpiArgs ea{q, nullptr, nullptr, nullptr, HD, 0};
    k_gemm<64, 128, 2, 2, EPI_BF16, 3><<<dim3(HD / 128, S_ / 64, 1), 256, 3 * 24576, stream>>>(h1, wq, ea, D_, HD, D_, D_); }
  k_attn<<<256, 512, SHM_ATTN, stream>>>(q, kc, vc, o);
  { EpiArgs ea{part, nullptr, nullptr, nullptr, D_, 0};
    k_gemm<64, 128, 2, 2, EPI_F32, 3><<<dim3(D_ / 128, S_ / 64, 4), 256, 3 * 24576, stream>>>(o, wo, ea, HD, D_, HD, HD / 4); }
  k_resid_ln2<4><<<S_ / 4, 256, 0, stream>>>(x, part, mod, x1, h2);
  { EpiArgs ea{z, nullptr, nullptr, nullptr, F_, 0};
    k_gemm<64, 128, 2, 2, EPI_GELU, 3><<<dim3(F_ / 128, S_ / 64, 1), 256, 3 * 24576, stream>>>(h2, w_in, ea, D_, F_, D_, D_); }
  k_ln_z2<<<S_ / 4, 256, 0, stream>>>(z, z2n);
  { EpiArgs ea{u, z, b_sp, nullptr, FH, 0};
    k_gemm<64, 128, 2, 2, EPI_SP, 4><<<dim3(FH / 128, S_ / 64, 1), 256, 4 * 24576, stream>>>(w_sp, z2n, ea, S_, FH, S_, S_); }
  { EpiArgs ea{out, x1, nullptr, mod + 5 * D_, D_, 0};
    k_gemm<64, 64, 2, 2, EPI_OUT, 4><<<dim3(D_ / 64, S_ / 64, 1), 256, 4 * 16384, stream>>>(u, w_out, ea, FH, D_, FH, FH); }
}
```

```cpp
#include <hip/hip_runtime.h>
#include <hip/hip_bf16.h>
#include <stdint.h>

typedef unsigned short u16;
typedef __attribute__((ext_vector_type(8))) short bf16x8;
typedef __attribute__((ext_vector_type(4))) short s16x4;
typedef __attribute__((ext_vector_type(16))) float f32x16;
typedef __attribute__((ext_vector_type(4))) unsigned u32x4;

constexpr int S_ = 2048, D_ = 512, H_ = 32, DH = 128, HD = H_ * DH;
constexpr int F_ = 1792, FH = 896;

__device__ __forceinline__ float bf2f(u16 v) { return __uint_as_float((unsigned)v << 16); }
__device__ __forceinline__ u16 f2bf(float f) {
  unsigned u = __float_as_uint(f);
  u += 0x7fffu + ((u >> 16) & 1u);
  return (u16)(u >> 16);
}
__device__ __forceinline__ float rbf(float f) { return bf2f(f2bf(f)); }
__device__ __forceinline__ unsigned cvtpk(float lo, float hi) {
  unsigned r; asm volatile("v_cvt_pk_bf16_f32 %0, %1, %2" : "=v"(r) : "v"(lo), "v"(hi)); return r;
}
__device__ __forceinline__ int crow(int r, int hi) { return (r & 3) + 8 * (r >> 2) + 4 * hi; }
typedef __attribute__((ext_vector_type(4))) __bf16 bfv4;
typedef __attribute__((address_space(3))) bfv4 lds_bfv4;
__device__ __forceinline__ s16x4 tr_read_b(const void* lds_ptr) {
  bfv4 v = __builtin_amdgcn_ds_read_tr16_b64_v4bf16((lds_bfv4*)(lds_ptr));
  return *reinterpret_cast<s16x4*>(&v);
}
__device__ __forceinline__ float wave_sum(float v) {
#pragma unroll
  for (int o = 32; o >= 1; o >>= 1) v += __shfl_xor(v, o, 64);
  return v;
}

__global__ __launch_bounds__(256) void k_mod(const u16* __restrict__ c, const u16* __restrict__ w_mod, float* __restrict__ mod) {
  __shared__ float red[32][65];
  const int cg = threadIdx.x & 7, kg = threadIdx.x >> 3;
  float acc[8];
#pragma unroll
  for (int j = 0; j < 8; ++j) acc[j] = 0.f;
  bf16x8 wv[16];
#pragma unroll
  for (int k = 0; k < 16; ++k) wv[k] = *reinterpret_cast<const bf16x8*>(w_mod + (long)(kg * 16 + k) * 3072 + blockIdx.x * 64 + cg * 8);
#pragma unroll
  for (int k = 0; k < 16; ++k) {
    float cv = bf2f(c[kg * 16 + k]);
    float sv = rbf(cv / (1.f + __expf(-cv)));
#pragma unroll
    for (int j = 0; j < 8; ++j) acc[j] += sv * bf2f((u16)wv[k][j]);
  }
#pragma unroll
  for (int j = 0; j < 8; ++j) red[kg][cg * 8 + j] = acc[j];
  __syncthreads();
  if (threadIdx.x < 64) {
    float t = 0.f;
#pragma unroll
    for (int g = 0; g < 32; ++g) t += red[g][threadIdx.x];
    mod[blockIdx.x * 64 + threadIdx.x] = rbf(t);
  }
}

__global__ __launch_bounds__(256) void k_ln1(const u16* __restrict__ x, const float* __restrict__ mod, u16* __restrict__ h) {
  const int lane = threadIdx.x & 63, row = blockIdx.x * 4 + (threadIdx.x >> 6);
  bf16x8 xv = *reinterpret_cast<const bf16x8*>(x + (long)row * D_ + lane * 8);
  float v[8]; float s = 0.f;
#pragma unroll
  for (int j = 0; j < 8; ++j) { v[j] = bf2f((u16)xv[j]); s += v[j]; }
  float mean = wave_sum(s) * (1.f / D_);
  float q = 0.f;
#pragma unroll
  for (int j = 0; j < 8; ++j) { float d = v[j] - mean; q += d * d; }
  float rstd = rsqrtf(wave_sum(q) * (1.f / D_) + 1e-5f);
  bf16x8 o;
#pragma unroll
  for (int j = 0; j < 8; ++j) {
    int cidx = lane * 8 + j;
    o[j] = (short)f2bf((v[j] - mean) * rstd * (1.f + mod[D_ + cidx]) + mod[cidx]);
  }
  *reinterpret_cast<bf16x8*>(h + (long)row * D_ + lane * 8) = o;
}

enum { EPI_BF16 = 0, EPI_F32 = 1, EPI_GELU = 2, EPI_SP = 3, EPI_OUT = 4 };
struct EpiArgs {
  void* C;
  const u16* aux0;
  const u16* aux1;
  const float* auxf;
  int ldc;
  int pad;
};

__device__ __forceinline__ float gelu_tanh(float x) {
  const float k0 = 0.7978845608028654f, k1 = 0.044715f;
  float u = k0 * (x + k1 * x * x * x);
  float e = __expf(2.f * u);
  float t = 1.f - 2.f / (e + 1.f);
  return 0.5f * x * (1.f + t);
}

template <int N> __device__ __forceinline__ void wait_vm() { asm volatile("s_waitcnt vmcnt(%0)" :: "n"(N) : "memory"); }
__device__ __forceinline__ void glds16(const void* gsrc, unsigned lds_dst) {
  unsigned keep;
  asm volatile("s_mov_b32 %0, m0\n\ts_mov_b32 m0, %2\n\ts_nop 0\n\tglobal_load_lds_dwordx4 %1, off\n\ts_mov_b32 m0, %0"
               : "=&s"(keep) : "v"(gsrc), "s"(lds_dst) : "memory");
}
template <int BM, int BN, int WM, int WN, int EPI, int NST>
__global__ __launch_bounds__(WM * WN * 64) void k_gemm(const u16* __restrict__ A, const u16* __restrict__ B, EpiArgs ea,
                                                      int lda, int ldb, int K, int ksplit) {
  constexpr int BK = 64, NT = WM * WN * 64;
  constexpr int TM = BM / WM / 32, TN = BN / WN / 32;
  constexpr int ACH = BM * 8 / NT, BCH = BN * 8 / NT, G = ACH + BCH;
  constexpr int ABYTES = BM * BK * 2, BBYTES = BK * BN * 2, STB = ABYTES + BBYTES;
  extern __shared__ __attribute__((aligned(16))) char smem[];
  const int tid = threadIdx.x, lane = tid & 63, wid = tid >> 6;
  const int wm = wid / WN, wn = wid % WN;
  const int r32 = lane & 31, hi = lane >> 5;
  const int bm = blockIdx.y * BM, bn = blockIdx.x * BN;
  const int kbeg = blockIdx.z * ksplit;
  const int nt = ksplit / BK;
  A += (long)bm * lda + kbeg;
  B += (long)kbeg * ldb + bn;
  const unsigned lds0 = (unsigned)(uintptr_t)smem;
  const unsigned wid_u = __builtin_amdgcn_readfirstlane(wid);
  auto stage = [&](int t, int buf) {
    const unsigned sa = lds0 + buf * STB + wid_u * 1024;
    const unsigned sb = sa + ABYTES;
#pragma unroll
    for (int i = 0; i < ACH; ++i) {
      int c = tid + i * NT; int row = c >> 3, ch = (c & 7) ^ ((row >> 1) & 7);
      glds16(A + (long)row * lda + t * BK + ch * 8, sa + i * NT * 16);
    }
#pragma unroll
    for (int i = 0; i < BCH; ++i) {
      int c = tid + i * NT; int panel = c >> 8, row = (c & 255) >> 2, ch = c & 3;
      glds16(B + (long)(t * BK + row) * ldb + panel * 32 + ch * 8, sb + i * NT * 16);
    }
  };
  f32x16 acc[TM][TN];
#pragma unroll
  for (int m = 0; m < TM; ++m)
#pragma unroll
    for (int n = 0; n < TN; ++n) acc[m][n] = f32x16{};
#pragma unroll
  for (int s0 = 0; s0 < NST - 1; ++s0) stage(s0, s0);
  const int q4 = (lane & 15) >> 2, p4 = lane & 3, blk = (lane >> 4) & 1;
  int buf = 0, nbuf = NST - 1;
  for (int t = 0; t < nt; ++t) {
    if (t + NST - 2 < nt) wait_vm<(NST - 2) * G>(); else wait_vm<0>();
    __builtin_amdgcn_s_barrier();
    if (t + NST - 1 < nt) stage(t + NST - 1, nbuf);
    const char* sa = smem + buf * STB;
    const char* sb = sa + ABYTES;
#pragma unroll
    for (int s = 0; s < 4; ++s) {
      bf16x8 af[TM], bfr[TN];
#pragma unroll
      for (int m = 0; m < TM; ++m) {
        int row = (wm * TM + m) * 32 + r32; int ch = 2 * s + hi;
        af[m] = *reinterpret_cast<const bf16x8*>(sa + row * 128 + ((ch ^ ((row >> 1) & 7)) << 4));
      }
#pragma unroll
      for (int n = 0; n < TN; ++n) {
        const char* pb = sb + (wn * TN + n) * 4096 + (16 * s + 8 * hi + q4) * 64 + blk * 32 + p4 * 8;
        s16x4 lo = tr_read_b(pb), hh = tr_read_b(pb + 256);
        bfr[n] = (bf16x8){lo[0], lo[1], lo[2], lo[3], hh[0], hh[1], hh[2], hh[3]};
      }
#pragma unroll
      for (int m = 0; m < TM; ++m)
#pragma unroll
        for (int n = 0; n < TN; ++n)
          acc[m][n] = __builtin_amdgcn_mfma_f32_32x32x16_bf16(af[m], bfr[n], acc[m][n], 0, 0, 0);
    }
    asm volatile("s_waitcnt lgkmcnt(0)" ::: "memory");
    buf = (buf + 1 == NST) ? 0 : buf + 1;
    nbuf = (nbuf + 1 == NST) ? 0 : nbuf + 1;
  }
  constexpr int CLD = BN + 4;
  float* cs = reinterpret_cast<float*>(smem);
  __builtin_amdgcn_s_barrier();
#pragma unroll
  for (int m = 0; m < TM; ++m)
#pragma unroll
    for (int n = 0; n < TN; ++n)
#pragma unroll
      for (int r = 0; r < 16; ++r)
        cs[((wm * TM + m) * 32 + crow(r, hi)) * CLD + (wn * TN + n) * 32 + r32] = acc[m][n][r];
  __syncthreads();
  constexpr int NCH = BM * BN / 8 / NT;
  bf16x8 a0[NCH]; float bsp[NCH];
#pragma unroll
  for (int i = 0; i < NCH; ++i) {
    const int c = tid + i * NT, row = bm + c / (BN / 8), col = bn + (c % (BN / 8)) * 8;
    if constexpr (EPI == EPI_SP) { a0[i] = *reinterpret_cast<const bf16x8*>(ea.aux0 + (long)row * F_ + col); bsp[i] = bf2f(ea.aux1[row]); }
    if constexpr (EPI == EPI_OUT) a0[i] = *reinterpret_cast<const bf16x8*>(ea.aux0 + (long)row * D_ + col);
  }
#pragma unroll
  for (int i = 0; i < NCH; ++i) {
    const int c = tid + i * NT, lr = c / (BN / 8), lc = (c % (BN / 8)) * 8, row = bm + lr, col = bn + lc;
    const float4 u0 = *reinterpret_cast<const float4*>(cs + lr * CLD + lc), u1 = *reinterpret_cast<const float4*>(cs + lr * CLD + lc + 4);
    float v[8] = {u0.x, u0.y, u0.z, u0.w, u1.x, u1.y, u1.z, u1.w};
    if constexpr (EPI == EPI_F32) {
      float* dst = (float*)ea.C + ((long)blockIdx.z * S_ + row) * ea.ldc + col;
      *reinterpret_cast<float4*>(dst) = u0; *reinterpret_cast<float4*>(dst + 4) = u1;
    } else {
      bf16x8 o;
#pragma unroll
      for (int j = 0; j < 8; ++j) {
        float y;
        if constexpr (EPI == EPI_BF16) y = v[j];
        else if constexpr (EPI == EPI_GELU) y = gelu_tanh(rbf(v[j]));
        else if constexpr (EPI == EPI_SP) y = bf2f((u16)a0[i][j]) * rbf(rbf(v[j]) + bsp[i]);
        else y = bf2f((u16)a0[i][j]) + rbf(ea.auxf[col + j] * rbf(v[j]));
        o[j] = (short)f2bf(y);
      }
      *reinterpret_cast<bf16x8*>((u16*)ea.C + (long)row * ea.ldc + col) = o;
    }
  }
}

template <int NSPLIT>
__global__ __launch_bounds__(256) void k_resid_ln2(const u16* __restrict__ x, const float* __restrict__ part, const float* __restrict__ mod,
                                                   u16* __restrict__ x1o, u16* __restrict__ h2) {
  const int lane = threadIdx.x & 63, row = blockIdx.x * 4 + (threadIdx.x >> 6);
  bf16x8 xv = *reinterpret_cast<const bf16x8*>(x + (long)row * D_ + lane * 8);
  float a[8];
#pragma unroll
  for (int j = 0; j < 8; ++j) a[j] = 0.f;
#pragma unroll
  for (int p = 0; p < NSPLIT; ++p) {
    const float4* pp = reinterpret_cast<const float4*>(part + ((long)p * S_ + row) * D_ + lane * 8);
    float4 u0 = pp[0], u1 = pp[1];
    a[0] += u0.x; a[1] += u0.y; a[2] += u0.z; a[3] += u0.w; a[4] += u1.x; a[5] += u1.y; a[6] += u1.z; a[7] += u1.w;
  }
  float v[8]; float s = 0.f; bf16x8 xo;
#pragma unroll
  for (int j = 0; j < 8; ++j) {
    int cidx = lane * 8 + j;
    float t = rbf(bf2f((u16)xv[j]) + rbf(mod[2 * D_ + cidx] * rbf(a[j])));
    v[j] = t; s += t; xo[j] = (short)f2bf(t);
  }
  *reinterpret_cast<bf16x8*>(x1o + (long)row * D_ + lane * 8) = xo;
  float mean = wave_sum(s) * (1.f / D_);
  float q = 0.f;
#pragma unroll
  for (int j = 0; j < 8; ++j) { float d = v[j] - mean; q += d * d; }
  float rstd = rsqrtf(wave_sum(q) * (1.f / D_) + 1e-5f);
  bf16x8 o;
#pragma unroll
  for (int j = 0; j < 8; ++j) {
    int cidx = lane * 8 + j;
    o[j] = (short)f2bf((v[j] - mean) * rstd * (1.f + mod[4 * D_ + cidx]) + mod[3 * D_ + cidx]);
  }
  *reinterpret_cast<bf16x8*>(h2 + (long)row * D_ + lane * 8) = o;
}

__global__ __launch_bounds__(256) void k_ln_z2(const u16* __restrict__ z, u16* __restrict__ z2n) {
  const int lane = threadIdx.x & 63, row = blockIdx.x * 4 + (threadIdx.x >> 6);
  float v[16]; float s = 0.f; const bool act = lane < 56;
  bf16x8 c0 = {}, c1 = {};
  if (act) {
    c0 = *reinterpret_cast<const bf16x8*>(z + (long)row * F_ + FH + lane * 16);
    c1 = *reinterpret_cast<const bf16x8*>(z + (long)row * F_ + FH + lane * 16 + 8);
  }
#pragma unroll
  for (int j = 0; j < 8; ++j) { v[j] = bf2f((u16)c0[j]); v[8 + j] = bf2f((u16)c1[j]); s += v[j] + v[8 + j]; }
  float mean = wave_sum(s) * (1.f / FH);
  float q = 0.f;
  if (act) {
#pragma unroll
    for (int j = 0; j < 16; ++j) { float d = v[j] - mean; q += d * d; }
  }
  float rstd = rsqrtf(wave_sum(q) * (1.f / FH) + 1e-5f);
  if (act) {
    bf16x8 o0, o1;
#pragma unroll
    for (int j = 0; j < 8; ++j) { o0[j] = (short)f2bf((v[j] - mean) * rstd); o1[j] = (short)f2bf((v[8 + j] - mean) * rstd); }
    *reinterpret_cast<bf16x8*>(z2n + (long)row * FH + lane * 16) = o0;
    *reinterpret_cast<bf16x8*>(z2n + (long)row * FH + lane * 16 + 8) = o1;
  }
}

constexpr int NW = 8, QBLK = 32, KVBLK = 64;
constexpr float SCALE = 0.088388347648318440f;
constexpr float THR = 8.f;
constexpr size_t SHM_V = KVBLK * DH * 2, SHM_K = KVBLK * DH * 2, SHM_ATTN = 2 * SHM_V + 2 * SHM_K + NW * 64 * 4;
#define KSWZ(row, colB) ((row) * 256 + ((colB) ^ (((row) & 7) << 4)))
#define SBAR() __builtin_amdgcn_sched_barrier(0)

__device__ __forceinline__ void partialSM(f32x16& p0, f32x16& p1, float& m_reg, float& mn, float& alpha) {
  constexpr float C = SCALE * 1.4426950408889634f;
  float pmax = p0[0];
  for (int r = 1; r < 16; ++r) pmax = fmaxf(pmax, p0[r]);
  for (int r = 0; r < 16; ++r) pmax = fmaxf(pmax, p1[r]);
  { auto rr = __builtin_amdgcn_permlane32_swap(__float_as_uint(pmax), __float_as_uint(pmax), false, false);
    pmax = fmaxf(__uint_as_float(rr[0]), __uint_as_float(rr[1])); }
  if (__builtin_expect(__all(pmax - m_reg <= THR / SCALE), 1)) { mn = m_reg; alpha = 1.f; }
  else { mn = fmaxf(m_reg, pmax); alpha = __builtin_amdgcn_exp2f((m_reg - mn) * C); m_reg = mn; }
  float mnC = -mn * C;
  for (int r = 0; r < 16; ++r) p0[r] = fmaf(p0[r], C, mnC);
  for (int r = 0; r < 16; ++r) p1[r] = fmaf(p1[r], C, mnC);
  for (int r = 0; r < 16; ++r) p0[r] = __builtin_amdgcn_exp2f(p0[r]);
}
__device__ __forceinline__ void finishSM(f32x16& p0, f32x16& p1, float alpha, float& l_reg, bf16x8& pa0, bf16x8& pa1, bf16x8& pa2, bf16x8& pa3) {
  for (int r = 0; r < 16; ++r) p1[r] = __builtin_amdgcn_exp2f(p1[r]);
  float ps = 0;
  for (int r = 0; r < 16; ++r) ps += p0[r];
  for (int r = 0; r < 16; ++r) ps += p1[r];
  { auto rr = __builtin_amdgcn_permlane32_swap(__float_as_uint(ps), __float_as_uint(ps), false, false);
    ps = __uint_as_float(rr[0]) + __uint_as_float(rr[1]); }
  l_reg = l_reg * alpha + ps;
#define PK4(P, BASE, OUT) do { unsigned a0 = cvtpk(P[BASE + 0], P[BASE + 1]), a1 = cvtpk(P[BASE + 2], P[BASE + 3]);   \
    unsigned b0 = cvtpk(P[BASE + 4], P[BASE + 5]), b1 = cvtpk(P[BASE + 6], P[BASE + 7]);                              \
    auto r0 = __builtin_amdgcn_permlane32_swap(a0, b0, false, false); auto r1 = __builtin_amdgcn_permlane32_swap(a1, b1, false, false); \
    u32x4 w = {r0[0], r1[0], r0[1], r1[1]}; OUT = *reinterpret_cast<bf16x8*>(&w); } while (0)
  PK4(p0, 0, pa0); PK4(p0, 8, pa1); PK4(p1, 0, pa2); PK4(p1, 8, pa3);
#undef PK4
}
__device__ __forceinline__ void qkt(f32x16& p0, f32x16& p1, const u16* Ks, const bf16x8* qr, int r32, int hi) {
  p0 = f32x16{}; p1 = f32x16{};
  for (int d0 = 0; d0 < 8; ++d0) { int cb = (d0 * 16 + hi * 8) * 2;
    bf16x8 b0 = *reinterpret_cast<const bf16x8*>((const char*)Ks + KSWZ(r32, cb));
    bf16x8 b1 = *reinterpret_cast<const bf16x8*>((const char*)Ks + KSWZ(32 + r32, cb));
    p0 = __builtin_amdgcn_mfma_f32_32x32x16_bf16(b0, qr[d0], p0, 0, 0, 0);
    p1 = __builtin_amdgcn_mfma_f32_32x32x16_bf16(b1, qr[d0], p1, 0, 0, 0); }
}
__device__ __forceinline__ int v_st(int k, int c) { const int kk = (k & ~0xC) | ((k & 4) << 1) | ((k & 8) >> 1); return ((kk >> 3) * 4 + (c >> 5)) * 512 + ((kk & 7) * 32 + (c & 31)) * 2; }
__device__ __forceinline__ int v_rd_base(int lane) { return ((lane & 3) << 3) | (((lane >> 2) & 3) << 6) | (((lane >> 4) & 1) << 5) | (((lane >> 5) & 1) << 8); }
constexpr int v_rd_off(int d0, int ks, int half) { return d0 * 512 + ks * 4096 + half * 2048; }
template <int OFF> __device__ __forceinline__ s16x4 tr_read(int vb) {
  s16x4 r; asm volatile("ds_read_b64_tr_b16 %0, %1 offset:%2" : "=&v"(r) : "v"(vb), "i"(OFF) : "memory"); return r;
}
template <int D0> __device__ __forceinline__ void pv_one(f32x16& od, int vb, bf16x8 pa0, bf16x8 pa1, bf16x8 pa2, bf16x8 pa3) {
  const s16x4 l0 = tr_read<v_rd_off(D0, 0, 0)>(vb), h0 = tr_read<v_rd_off(D0, 0, 1)>(vb), l1 = tr_read<v_rd_off(D0, 1, 0)>(vb), h1 = tr_read<v_rd_off(D0, 1, 1)>(vb);
  const s16x4 l2 = tr_read<v_rd_off(D0, 2, 0)>(vb), h2 = tr_read<v_rd_off(D0, 2, 1)>(vb), l3 = tr_read<v_rd_off(D0, 3, 0)>(vb), h3 = tr_read<v_rd_off(D0, 3, 1)>(vb);
  asm volatile("s_waitcnt lgkmcnt(0)" ::: "memory"); SBAR();
#define PK(L, H) (bf16x8){L[0], L[1], L[2], L[3], H[0], H[1], H[2], H[3]}
  od = __builtin_amdgcn_mfma_f32_32x32x16_bf16(pa0, PK(l0, h0), od, 0, 0, 0);
  od = __builtin_amdgcn_mfma_f32_32x32x16_bf16(pa1, PK(l1, h1), od, 0, 0, 0);
  od = __builtin_amdgcn_mfma_f32_32x32x16_bf16(pa2, PK(l2, h2), od, 0, 0, 0);
  od = __builtin_amdgcn_mfma_f32_32x32x16_bf16(pa3, PK(l3, h3), od, 0, 0, 0);
#undef PK
}
__device__ __forceinline__ void pv_d0(f32x16* o, int vb, bf16x8 pa0, bf16x8 pa1, bf16x8 pa2, bf16x8 pa3) {
  pv_one<0>(o[0], vb, pa0, pa1, pa2, pa3); pv_one<1>(o[1], vb, pa0, pa1, pa2, pa3); pv_one<2>(o[2], vb, pa0, pa1, pa2, pa3); pv_one<3>(o[3], vb, pa0, pa1, pa2, pa3);
}

__global__ __launch_bounds__(512, 1) void k_attn(const u16* __restrict__ Q, const u16* __restrict__ Kc, const u16* __restrict__ Vc, u16* __restrict__ O) {
  extern __shared__ __attribute__((aligned(16))) char lds[];
  const int b = blockIdx.x;
  const int head = (b & 7) + 8 * ((b >> 3) & 3), qb = b >> 5;
  const u16* Qb = Q + (long)qb * 256 * HD + head * DH;
  const u16* Kh = Kc + (long)head * S_ * DH;
  const u16* Vh = Vc + (long)head * S_ * DH;
  u16* Ob = O + (long)qb * 256 * HD + head * DH;
  const int tid = threadIdx.x, wid = tid >> 6, lane = tid & 63, r32 = lane & 31, hi = lane >> 5;
  u16* V_lds = (u16*)lds; u16* K_lds = (u16*)(lds + 2 * SHM_V);
  float* ws = (float*)(lds + 2 * SHM_V + 2 * SHM_K) + wid * 64; float* li_l = ws; float* al_l = ws + 32;
  float m_reg = -1e30f, l_reg = 0; f32x16 o[4] = {}; bf16x8 qr[8];
  const u16* Qw = Qb + (long)(wid * QBLK + r32) * HD + hi * 8;
#pragma unroll
  for (int d0 = 0; d0 < 8; ++d0) qr[d0] = *reinterpret_cast<const bf16x8*>(Qw + d0 * 16);
  const int sr = tid >> 4, sc = (tid & 15) * 8, vst0 = v_st(sr, sc), vst1 = v_st(32 + sr, sc);
  const int vb0 = (int)(uintptr_t)V_lds + v_rd_base(lane);
  struct { bf16x8 vs0, vs1, ks0, ks1; } sr_[2];
#define LD8(p) (*reinterpret_cast<const bf16x8*>(p))
#define SLOAD(i, k0) do { sr_[i].vs0 = LD8(&Vh[(long)((k0) + sr) * DH + sc]); sr_[i].vs1 = LD8(&Vh[(long)((k0) + 32 + sr) * DH + sc]); \
    sr_[i].ks0 = LD8(&Kh[(long)((k0) + sr) * DH + sc]); sr_[i].ks1 = LD8(&Kh[(long)((k0) + 32 + sr) * DH + sc]); } while (0)
#define SWRITE(b, i) do { *(bf16x8*)((char*)V_lds + (b) * SHM_V + vst0) = sr_[i].vs0;          \
    *(bf16x8*)((char*)V_lds + (b) * SHM_V + vst1) = sr_[i].vs1; int kc = sc * 2;               \
    *(bf16x8*)((char*)K_lds + (b) * SHM_K + KSWZ(sr, kc)) = sr_[i].ks0;                       \
    *(bf16x8*)((char*)K_lds + (b) * SHM_K + KSWZ(32 + sr, kc)) = sr_[i].ks1; } while (0)
#define SWAIT() asm volatile("s_waitcnt vmcnt(4)" ::: "memory")
#define RESC(a) do { if (__any((a) < 1.f)) { if (hi == 0) al_l[r32] = (a); asm volatile("s_waitcnt lgkmcnt(0)" ::: "memory"); \
    for (int d = 0; d < 4; ++d) for (int r = 0; r < 16; ++r) o[d][r] *= al_l[crow(r, hi)]; } } while (0)
  f32x16 pA0, pA1, pB0, pB1; float mnA, mnB, alA, alB; bf16x8 pa0, pa1, pa2, pa3; constexpr int NT = S_ / KVBLK;
  constexpr int SE = 0, SO = 1;
  SLOAD(SE, 0); asm volatile("s_waitcnt vmcnt(0)" ::: "memory"); SWRITE(0, SE); __syncthreads();
  qkt(pA0, pA1, K_lds, qr, r32, hi); partialSM(pA0, pA1, m_reg, mnA, alA);
  SLOAD(SO, KVBLK); SLOAD(SE, 2 * KVBLK);
  SWAIT(); SWRITE(1, SO); __syncthreads();
  for (int j = 1; j + 1 < NT; j += 2) {
    SBAR(); qkt(pB0, pB1, (u16*)((char*)K_lds + SHM_K), qr, r32, hi);
    finishSM(pA0, pA1, alA, l_reg, pa0, pa1, pa2, pa3); SBAR();
    SLOAD(SO, (j + 2) * KVBLK); SBAR();
    pv_d0(o, vb0, pa0, pa1, pa2, pa3); partialSM(pB0, pB1, m_reg, mnB, alB);
    __syncthreads(); SWAIT(); SWRITE(0, SE);
    RESC(alB); __syncthreads();
    SBAR(); qkt(pA0, pA1, K_lds, qr, r32, hi);
    finishSM(pB0, pB1, alB, l_reg, pa0, pa1, pa2, pa3); SBAR();
    if (j + 3 < NT) SLOAD(SE, (j + 3) * KVBLK); SBAR();
    pv_d0(o, vb0 + (int)SHM_V, pa0, pa1, pa2, pa3); partialSM(pA0, pA1, m_reg, mnA, alA);
    __syncthreads(); SWAIT(); SWRITE(1, SO);
    RESC(alA); __syncthreads();
  }
  SBAR(); qkt(pB0, pB1, (u16*)((char*)K_lds + SHM_K), qr, r32, hi);
  finishSM(pA0, pA1, alA, l_reg, pa0, pa1, pa2, pa3); SBAR();
  pv_d0(o, vb0, pa0, pa1, pa2, pa3); partialSM(pB0, pB1, m_reg, mnB, alB);
  __syncthreads(); RESC(alB);
  finishSM(pB0, pB1, alB, l_reg, pa0, pa1, pa2, pa3); SBAR();
  pv_d0(o, vb0 + (int)SHM_V, pa0, pa1, pa2, pa3);
  if (hi == 0) li_l[r32] = l_reg; asm volatile("s_waitcnt lgkmcnt(0)" ::: "memory");
  float rli[16];
#pragma unroll
  for (int r = 0; r < 16; ++r) rli[r] = __builtin_amdgcn_rcpf(li_l[crow(r, hi)]);
  u16* Ow = Ob + (long)(wid * QBLK) * HD;
#pragma unroll
  for (int r = 0; r < 16; ++r) { int orow = crow(r, hi);
#pragma unroll
    for (int d0 = 0; d0 < 4; ++d0) Ow[(long)orow * HD + d0 * 32 + r32] = f2bf(o[d0][r] * rli[r]); }
#undef SLOAD
#undef SWRITE
#undef SWAIT
#undef RESC
}

extern "C" void kernel_launch(void* const* d_in, const int* in_sizes, int n_in, void* d_out, int out_size, void* d_ws, size_t ws_size,
                              hipStream_t stream) {
  const u16* x = (const u16*)d_in[0];
  const u16* kc = (const u16*)d_in[1];
  const u16* vc = (const u16*)d_in[2];
  const u16* wq = (const u16*)d_in[3];
  const u16* wo = (const u16*)d_in[4];
  const u16* w_in = (const u16*)d_in[6];
  const u16* w_sp = (const u16*)d_in[7];
  const u16* b_sp = (const u16*)d_in[8];
  const u16* w_out = (const u16*)d_in[9];
  const u16* c = (const u16*)d_in[10];
  const u16* w_mod = (const u16*)d_in[11];
  u16* out = (u16*)d_out;

  char* ws = (char*)d_ws;
  size_t off = 0;
  auto carve = [&](size_t bytes) { void* p = ws + off; off += (bytes + 255) / 256 * 256; return p; };
  float* mod = (float*)carve(3072 * 4);
  u16* h1 = (u16*)carve((size_t)S_ * D_ * 2);
  u16* q = (u16*)carve((size_t)S_ * HD * 2);
  u16* o = (u16*)carve((size_t)S_ * HD * 2);
  float* part = (float*)carve((size_t)4 * S_ * D_ * 4);
  u16* x1 = (u16*)carve((size_t)S_ * D_ * 2);
  u16* h2 = (u16*)carve((size_t)S_ * D_ * 2);
  u16* z = (u16*)carve((size_t)S_ * F_ * 2);
  u16* z2n = (u16*)carve((size_t)S_ * FH * 2);
  u16* u = (u16*)carve((size_t)S_ * FH * 2);

  constexpr int GS = 2 * (128 * 64 * 2 + 64 * 128 * 2);
  hipFuncSetAttribute((const void*)k_gemm<64, 128, 2, 2, EPI_BF16, 3>, hipFuncAttributeMaxDynamicSharedMemorySize, 3 * 24576);
  hipFuncSetAttribute((const void*)k_gemm<64, 128, 2, 2, EPI_F32, 3>, hipFuncAttributeMaxDynamicSharedMemorySize, 3 * 24576);
  hipFuncSetAttribute((const void*)k_gemm<64, 128, 2, 2, EPI_GELU, 3>, hipFuncAttributeMaxDynamicSharedMemorySize, 3 * 24576);
  hipFuncSetAttribute((const void*)k_gemm<64, 128, 2, 2, EPI_SP, 4>, hipFuncAttributeMaxDynamicSharedMemorySize, 4 * 24576);
  hipFuncSetAttribute((const void*)k_gemm<64, 64, 2, 2, EPI_OUT, 4>, hipFuncAttributeMaxDynamicSharedMemorySize, 4 * 16384);
  hipFuncSetAttribute((const void*)k_attn, hipFuncAttributeMaxDynamicSharedMemorySize, (int)SHM_ATTN);

  k_mod<<<48, 256, 0, stream>>>(c, w_mod, mod);
  k_ln1<<<S_ / 4, 256, 0, stream>>>(x, mod, h1);
  { EpiArgs ea{q, nullptr, nullptr, nullptr, HD, 0};
    k_gemm<64, 128, 2, 2, EPI_BF16, 3><<<dim3(HD / 128, S_ / 64, 1), 256, 3 * 24576, stream>>>(h1, wq, ea, D_, HD, D_, D_); }
  k_attn<<<256, 512, SHM_ATTN, stream>>>(q, kc, vc, o);
  { EpiArgs ea{part, nullptr, nullptr, nullptr, D_, 0};
    k_gemm<64, 128, 2, 2, EPI_F32, 3><<<dim3(D_ / 128, S_ / 64, 4), 256, 3 * 24576, stream>>>(o, wo, ea, HD, D_, HD, HD / 4); }
  k_resid_ln2<4><<<S_ / 4, 256, 0, stream>>>(x, part, mod, x1, h2);
  { EpiArgs ea{z, nullptr, nullptr, nullptr, F_, 0};
    k_gemm<64, 128, 2, 2, EPI_GELU, 3><<<dim3(F_ / 128, S_ / 64, 1), 256, 3 * 24576, stream>>>(h2, w_in, ea, D_, F_, D_, D_); }
  k_ln_z2<<<S_ / 4, 256, 0, stream>>>(z, z2n);
  { EpiArgs ea{u, z, b_sp, nullptr, FH, 0};
    k_gemm<64, 128, 2, 2, EPI_SP, 4><<<dim3(FH / 128, S_ / 64, 1), 256, 4 * 24576, stream>>>(w_sp, z2n, ea, S_, FH, S_, S_); }
  { EpiArgs ea{out, x1, nullptr, mod + 5 * D_, D_, 0};
    k_gemm<64, 64, 2, 2, EPI_OUT, 4><<<dim3(D_ / 64, S_ / 64, 1), 256, 4 * 16384, stream>>>(u, w_out, ea, FH, D_, FH, FH); }
}
```

```cpp
#include <hip/hip_runtime.h>
#include <hip/hip_bf16.h>
#include <stdint.h>

typedef unsigned short u16;
typedef __attribute__((ext_vector_type(8))) short bf16x8;
typedef __attribute__((ext_vector_type(4))) short s16x4;
typedef __attribute__((ext_vector_type(16))) float f32x16;
typedef __attribute__((ext_vector_type(4))) unsigned u32x4;

constexpr int S_ = 2048, D_ = 512, H_ = 32, DH = 128, HD = H_ * DH;
constexpr int F_ = 1792, FH = 896;

__device__ __forceinline__ float bf2f(u16 v) { return __uint_as_float((unsigned)v << 16); }
__device__ __forceinline__ u16 f2bf(float f) {
  unsigned u = __float_as_uint(f);
  u += 0x7fffu + ((u >> 16) & 1u);
  return (u16)(u >> 16);
}
__device__ __forceinline__ float rbf(float f) { return bf2f(f2bf(f)); }
__device__ __forceinline__ unsigned cvtpk(float lo, float hi) {
  unsigned r; asm volatile("v_cvt_pk_bf16_f32 %0, %1, %2" : "=v"(r) : "v"(lo), "v"(hi)); return r;
}
__device__ __forceinline__ int crow(int r, int hi) { return (r & 3) + 8 * (r >> 2) + 4 * hi; }
typedef __attribute__((ext_vector_type(4))) __bf16 bfv4;
typedef __attribute__((address_space(3))) bfv4 lds_bfv4;
__device__ __forceinline__ s16x4 tr_read_b(const void* lds_ptr) {
  bfv4 v = __builtin_amdgcn_ds_read_tr16_b64_v4bf16((lds_bfv4*)(lds_ptr));
  return *reinterpret_cast<s16x4*>(&v);
}
__device__ __forceinline__ float wave_sum(float v) {
#pragma unroll
  for (int o = 32; o >= 1; o >>= 1) v += __shfl_xor(v, o, 64);
  return v;
}

__global__ __launch_bounds__(256) void k_mod(const u16* __restrict__ c, const u16* __restrict__ w_mod, float* __restrict__ mod) {
  __shared__ float red[32][65];
  const int cg = threadIdx.x & 7, kg = threadIdx.x >> 3;
  float acc[8];
#pragma unroll
  for (int j = 0; j < 8; ++j) acc[j] = 0.f;
  bf16x8 wv[16];
#pragma unroll
  for (int k = 0; k < 16; ++k) wv[k] = *reinterpret_cast<const bf16x8*>(w_mod + (long)(kg * 16 + k) * 3072 + blockIdx.x * 64 + cg * 8);
#pragma unroll
  for (int k = 0; k < 16; ++k) {
    float cv = bf2f(c[kg * 16 + k]);
    float sv = rbf(cv / (1.f + __expf(-cv)));
#pragma unroll
    for (int j = 0; j < 8; ++j) acc[j] += sv * bf2f((u16)wv[k][j]);
  }
#pragma unroll
  for (int j = 0; j < 8; ++j) red[kg][cg * 8 + j] = acc[j];
  __syncthreads();
  if (threadIdx.x < 64) {
    float t = 0.f;
#pragma unroll
    for (int g = 0; g < 32; ++g) t += red[g][threadIdx.x];
    mod[blockIdx.x * 64 + threadIdx.x] = rbf(t);
  }
}

__global__ __launch_bounds__(256) void k_ln1(const u16* __restrict__ x, const float* __restrict__ mod, u16* __restrict__ h) {
  const int lane = threadIdx.x & 63, row = blockIdx.x * 4 + (threadIdx.x >> 6);
  bf16x8 xv = *reinterpret_cast<const bf16x8*>(x + (long)row * D_ + lane * 8);
  float v[8]; float s = 0.f;
#pragma unroll
  for (int j = 0; j < 8; ++j) { v[j] = bf2f((u16)xv[j]); s += v[j]; }
  float mean = wave_sum(s) * (1.f / D_);
  float q = 0.f;
#pragma unroll
  for (int j = 0; j < 8; ++j) { float d = v[j] - mean; q += d * d; }
  float rstd = rsqrtf(wave_sum(q) * (1.f / D_) + 1e-5f);
  bf16x8 o;
#pragma unroll
  for (int j = 0; j < 8; ++j) {
    int cidx = lane * 8 + j;
    o[j] = (short)f2bf((v[j] - mean) * rstd * (1.f + mod[D_ + cidx]) + mod[cidx]);
  }
  *reinterpret_cast<bf16x8*>(h + (long)row * D_ + lane * 8) = o;
}

enum { EPI_BF16 = 0, EPI_F32 = 1, EPI_GELU = 2, EPI_SP = 3, EPI_OUT = 4 };
struct EpiArgs {
  void* C;
  const u16* aux0;
  const u16* aux1;
  const float* auxf;
  int ldc;
  int pad;
};

__device__ __forceinline__ float gelu_tanh(float x) {
  const float k0 = 0.7978845608028654f, k1 = 0.044715f;
  float u = k0 * (x + k1 * x * x * x);
  float e = __expf(2.f * u);
  float t = 1.f - 2.f / (e + 1.f);
  return 0.5f * x * (1.f + t);
}

template <int N> __device__ __forceinline__ void wait_vm() { asm volatile("s_waitcnt vmcnt(%0)" :: "n"(N) : "memory"); }
__device__ __forceinline__ void glds16(const void* gsrc, unsigned lds_dst) {
  unsigned keep;
  asm volatile("s_mov_b32 %0, m0\n\ts_mov_b32 m0, %2\n\ts_nop 0\n\tglobal_load_lds_dwordx4 %1, off\n\ts_mov_b32 m0, %0"
               : "=&s"(keep) : "v"(gsrc), "s"(lds_dst) : "memory");
}
template <int BM, int BN, int WM, int WN, int EPI, int NST>
__global__ __launch_bounds__(WM * WN * 64) void k_gemm(const u16* __restrict__ A, const u16* __restrict__ B, EpiArgs ea,
                                                      int lda, int ldb, int K, int ksplit) {
  constexpr int BK = 64, NT = WM * WN * 64;
  constexpr int TM = BM / WM / 32, TN = BN / WN / 32;
  constexpr int ACH = BM * 8 / NT, BCH = BN * 8 / NT, G = ACH + BCH;
  constexpr int ABYTES = BM * BK * 2, BBYTES = BK * BN * 2, STB = ABYTES + BBYTES;
  extern __shared__ __attribute__((aligned(16))) char smem[];
  const int tid = threadIdx.x, lane = tid & 63, wid = tid >> 6;
  const int wm = wid / WN, wn = wid % WN;
  const int r32 = lane & 31, hi = lane >> 5;
  const int bm = blockIdx.y * BM, bn = blockIdx.x * BN;
  const int kbeg = blockIdx.z * ksplit;
  const int nt = ksplit / BK;
  A += (long)bm * lda + kbeg;
  B += (long)kbeg * ldb + bn;
  const unsigned lds0 = (unsigned)(uintptr_t)smem;
  const unsigned wid_u = __builtin_amdgcn_readfirstlane(wid);
  auto stage = [&](int t, int buf) {
    const unsigned sa = lds0 + buf * STB + wid_u * 1024;
    const unsigned sb = sa + ABYTES;
#pragma unroll
    for (int i = 0; i < ACH; ++i) {
      int c = tid + i * NT; int row = c >> 3, ch = (c & 7) ^ ((row >> 1) & 7);
      glds16(A + (long)row * lda + t * BK + ch * 8, sa + i * NT * 16);
    }
#pragma unroll
    for (int i = 0; i < BCH; ++i) {
      int c = tid + i * NT; int panel = c >> 8, row = (c & 255) >> 2, ch = c & 3;
      glds16(B + (long)(t * BK + row) * ldb + panel * 32 + ch * 8, sb + i * NT * 16);
    }
  };
  f32x16 acc[TM][TN];
#pragma unroll
  for (int m = 0; m < TM; ++m)
#pragma unroll
    for (int n = 0; n < TN; ++n) acc[m][n] = f32x16{};
#pragma unroll
  for (int s0 = 0; s0 < NST - 1; ++s0) stage(s0, s0);
  const int q4 = (lane & 15) >> 2, p4 = lane & 3, blk = (lane >> 4) & 1;
  int buf = 0, nbuf = NST - 1;
  for (int t = 0; t < nt; ++t) {
    if (t + NST - 2 < nt) wait_vm<(NST - 2) * G>(); else wait_vm<0>();
    __builtin_amdgcn_s_barrier();
    if (t + NST - 1 < nt) stage(t + NST - 1, nbuf);
    const char* sa = smem + buf * STB;
    const char* sb = sa + ABYTES;
#pragma unroll
    for (int s = 0; s < 4; ++s) {
      bf16x8 af[TM], bfr[TN];
#pragma unroll
      for (int m = 0; m < TM; ++m) {
        int row = (wm * TM + m) * 32 + r32; int ch = 2 * s + hi;
        af[m] = *reinterpret_cast<const bf16x8*>(sa + row * 128 + ((ch ^ ((row >> 1) & 7)) << 4));
      }
#pragma unroll
      for (int n = 0; n < TN; ++n) {
        const char* pb = sb + (wn * TN + n) * 4096 + (16 * s + 8 * hi + q4) * 64 + blk * 32 + p4 * 8;
        s16x4 lo = tr_read_b(pb), hh = tr_read_b(pb + 256);
        bfr[n] = (bf16x8){lo[0], lo[1], lo[2], lo[3], hh[0], hh[1], hh[2], hh[3]};
      }
#pragma unroll
      for (int m = 0; m < TM; ++m)
#pragma unroll
        for (int n = 0; n < TN; ++n)
          acc[m][n] = __builtin_amdgcn_mfma_f32_32x32x16_bf16(af[m], bfr[n], acc[m][n], 0, 0, 0);
    }
    asm volatile("s_waitcnt lgkmcnt(0)" ::: "memory");
    buf = (buf + 1 == NST) ? 0 : buf + 1;
    nbuf = (nbuf + 1 == NST) ? 0 : nbuf + 1;
  }
  constexpr int CLD = BN + 4;
  float* cs = reinterpret_cast<float*>(smem);
  __builtin_amdgcn_s_barrier();
#pragma unroll
  for (int m = 0; m < TM; ++m)
#pragma unroll
    for (int n = 0; n < TN; ++n)
#pragma unroll
      for (int r = 0; r < 16; ++r)
        cs[((wm * TM + m) * 32 + crow(r, hi)) * CLD + (wn * TN + n) * 32 + r32] = acc[m][n][r];
  __syncthreads();
  constexpr int NCH = BM * BN / 8 / NT;
  bf16x8 a0[NCH]; float bsp[NCH];
#pragma unroll
  for (int i = 0; i < NCH; ++i) {
    const int c = tid + i * NT, row = bm + c / (BN / 8), col = bn + (c % (BN / 8)) * 8;
    if constexpr (EPI == EPI_SP) { a0[i] = *reinterpret_cast<const bf16x8*>(ea.aux0 + (long)row * F_ + col); bsp[i] = bf2f(ea.aux1[row]); }
    if constexpr (EPI == EPI_OUT) a0[i] = *reinterpret_cast<const bf16x8*>(ea.aux0 + (long)row * D_ + col);
  }
#pragma unroll
  for (int i = 0; i < NCH; ++i) {
    const int c = tid + i * NT, lr = c / (BN / 8), lc = (c % (BN / 8)) * 8, row = bm + lr, col = bn + lc;
    const float4 u0 = *reinterpret_cast<const float4*>(cs + lr * CLD + lc), u1 = *reinterpret_cast<const float4*>(cs + lr * CLD + lc + 4);
    float v[8] = {u0.x, u0.y, u0.z, u0.w, u1.x, u1.y, u1.z, u1.w};
    if constexpr (EPI == EPI_F32) {
      float* dst = (float*)ea.C + ((long)blockIdx.z * S_ + row) * ea.ldc + col;
      *reinterpret_cast<float4*>(dst) = u0; *reinterpret_cast<float4*>(dst + 4) = u1;
    } else {
      bf16x8 o;
#pragma unroll
      for (int j = 0; j < 8; ++j) {
        float y;
        if constexpr (EPI == EPI_BF16) y = v[j];
        else if constexpr (EPI == EPI_GELU) y = gelu_tanh(rbf(v[j]));
        else if constexpr (EPI == EPI_SP) y = bf2f((u16)a0[i][j]) * rbf(rbf(v[j]) + bsp[i]);
        else y = bf2f((u16)a0[i][j]) + rbf(ea.auxf[col + j] * rbf(v[j]));
        o[j] = (short)f2bf(y);
      }
      *reinterpret_cast<bf16x8*>((u16*)ea.C + (long)row * ea.ldc + col) = o;
    }
  }
}

template <int NSPLIT>
__global__ __launch_bounds__(256) void k_resid_ln2(const u16* __restrict__ x, const float* __restrict__ part, const float* __restrict__ mod,
                                                   u16* __restrict__ x1o, u16* __restrict__ h2) {
  const int lane = threadIdx.x & 63, row = blockIdx.x * 4 + (threadIdx.x >> 6);
  bf16x8 xv = *reinterpret_cast<const bf16x8*>(x + (long)row * D_ + lane * 8);
  float a[8];
#pragma unroll
  for (int j = 0; j < 8; ++j) a[j] = 0.f;
#pragma unroll
  for (int p = 0; p < NSPLIT; ++p) {
    const float4* pp = reinterpret_cast<const float4*>(part + ((long)p * S_ + row) * D_ + lane * 8);
    float4 u0 = pp[0], u1 = pp[1];
    a[0] += u0.x; a[1] += u0.y; a[2] += u0.z; a[3] += u0.w; a[4] += u1.x; a[5] += u1.y; a[6] += u1.z; a[7] += u1.w;
  }
  float v[8]; float s = 0.f; bf16x8 xo;
#pragma unroll
  for (int j = 0; j < 8; ++j) {
    int cidx = lane * 8 + j;
    float t = rbf(bf2f((u16)xv[j]) + rbf(mod[2 * D_ + cidx] * rbf(a[j])));
    v[j] = t; s += t; xo[j] = (short)f2bf(t);
  }
  *reinterpret_cast<bf16x8*>(x1o + (long)row * D_ + lane * 8) = xo;
  float mean = wave_sum(s) * (1.f / D_);
  float q = 0.f;
#pragma unroll
  for (int j = 0; j < 8; ++j) { float d = v[j] - mean; q += d * d; }
  float rstd = rsqrtf(wave_sum(q) * (1.f / D_) + 1e-5f);
  bf16x8 o;
#pragma unroll
  for (int j = 0; j < 8; ++j) {
    int cidx = lane * 8 + j;
    o[j] = (short)f2bf((v[j] - mean) * rstd * (1.f + mod[4 * D_ + cidx]) + mod[3 * D_ + cidx]);
  }
  *reinterpret_cast<bf16x8*>(h2 + (long)row * D_ + lane * 8) = o;
}

__global__ __launch_bounds__(256) void k_ln_z2(const u16* __restrict__ z, u16* __restrict__ z2n) {
  const int lane = threadIdx.x & 63, row = blockIdx.x * 4 + (threadIdx.x >> 6);
  float v[16]; float s = 0.f; const bool act = lane < 56;
  bf16x8 c0 = {}, c1 = {};
  if (act) {
    c0 = *reinterpret_cast<const bf16x8*>(z + (long)row * F_ + FH + lane * 16);
    c1 = *reinterpret_cast<const bf16x8*>(z + (long)row * F_ + FH + lane * 16 + 8);
  }
#pragma unroll
  for (int j = 0; j < 8; ++j) { v[j] = bf2f((u16)c0[j]); v[8 + j] = bf2f((u16)c1[j]); s += v[j] + v[8 + j]; }
  float mean = wave_sum(s) * (1.f / FH);
  float q = 0.f;
  if (act) {
#pragma unroll
    for (int j = 0; j < 16; ++j) { float d = v[j] - mean; q += d * d; }
  }
  float rstd = rsqrtf(wave_sum(q) * (1.f / FH) + 1e-5f);
  if (act) {
    bf16x8 o0, o1;
#pragma unroll
    for (int j = 0; j < 8; ++j) { o0[j] = (short)f2bf((v[j] - mean) * rstd); o1[j] = (short)f2bf((v[8 + j] - mean) * rstd); }
    *reinterpret_cast<bf16x8*>(z2n + (long)row * FH + lane * 16) = o0;
    *reinterpret_cast<bf16x8*>(z2n + (long)row * FH + lane * 16 + 8) = o1;
  }
}

constexpr int NW = 8, QBLK = 32, KVBLK = 64;
constexpr float SCALE = 0.088388347648318440f;
constexpr float THR = 8.f;
constexpr size_t SHM_V = KVBLK * DH * 2, SHM_K = KVBLK * DH * 2, SHM_ATTN = 2 * SHM_V + 2 * SHM_K + NW * 64 * 4;
#define KSWZ(row, colB) ((row) * 256 + ((colB) ^ (((row) & 7) << 4)))
#define SBAR() __builtin_amdgcn_sched_barrier(0)

__device__ __forceinline__ void partialSM(f32x16& p0, f32x16& p1, float& m_reg, float& mn, float& alpha) {
  constexpr float C = SCALE * 1.4426950408889634f;
  float pmax = p0[0];
  for (int r = 1; r < 16; ++r) pmax = fmaxf(pmax, p0[r]);
  for (int r = 0; r < 16; ++r) pmax = fmaxf(pmax, p1[r]);
  { auto rr = __builtin_amdgcn_permlane32_swap(__float_as_uint(pmax), __float_as_uint(pmax), false, false);
    pmax = fmaxf(__uint_as_float(rr[0]), __uint_as_float(rr[1])); }
  if (__builtin_expect(__all(pmax - m_reg <= THR / SCALE), 1)) { mn = m_reg; alpha = 1.f; }
  else { mn = fmaxf(m_reg, pmax); alpha = __builtin_amdgcn_exp2f((m_reg - mn) * C); m_reg = mn; }
  float mnC = -mn * C;
  for (int r = 0; r < 16; ++r) p0[r] = fmaf(p0[r], C, mnC);
  for (int r = 0; r < 16; ++r) p1[r] = fmaf(p1[r], C, mnC);
  for (int r = 0; r < 16; ++r) p0[r] = __builtin_amdgcn_exp2f(p0[r]);
}
__device__ __forceinline__ void finishSM(f32x16& p0, f32x16& p1, float alpha, float& l_reg, bf16x8& pa0, bf16x8& pa1, bf16x8& pa2, bf16x8& pa3) {
  for (int r = 0; r < 16; ++r) p1[r] = __builtin_amdgcn_exp2f(p1[r]);
  float ps = 0;
  for (int r = 0; r < 16; ++r) ps += p0[r];
  for (int r = 0; r < 16; ++r) ps += p1[r];
  { auto rr = __builtin_amdgcn_permlane32_swap(__float_as_uint(ps), __float_as_uint(ps), false, false);
    ps = __uint_as_float(rr[0]) + __uint_as_float(rr[1]); }
  l_reg = l_reg * alpha + ps;
#define PK4(P, BASE, OUT) do { unsigned a0 = cvtpk(P[BASE + 0], P[BASE + 1]), a1 = cvtpk(P[BASE + 2], P[BASE + 3]);   \
    unsigned b0 = cvtpk(P[BASE + 4], P[BASE + 5]), b1 = cvtpk(P[BASE + 6], P[BASE + 7]);                              \
    auto r0 = __builtin_amdgcn_permlane32_swap(a0, b0, false, false); auto r1 = __builtin_amdgcn_permlane32_swap(a1, b1, false, false); \
    u32x4 w = {r0[0], r1[0], r0[1], r1[1]}; OUT = *reinterpret_cast<bf16x8*>(&w); } while (0)
  PK4(p0, 0, pa0); PK4(p0, 8, pa1); PK4(p1, 0, pa2); PK4(p1, 8, pa3);
#undef PK4
}
__device__ __forceinline__ void qkt(f32x16& p0, f32x16& p1, const u16* Ks, const bf16x8* qr, int r32, int hi) {
  p0 = f32x16{}; p1 = f32x16{};
  for (int d0 = 0; d0 < 8; ++d0) { int cb = (d0 * 16 + hi * 8) * 2;
    bf16x8 b0 = *reinterpret_cast<const bf16x8*>((const char*)Ks + KSWZ(r32, cb));
    bf16x8 b1 = *reinterpret_cast<const bf16x8*>((const char*)Ks + KSWZ(32 + r32, cb));
    p0 = __builtin_amdgcn_mfma_f32_32x32x16_bf16(b0, qr[d0], p0, 0, 0, 0);
    p1 = __builtin_amdgcn_mfma_f32_32x32x16_bf16(b1, qr[d0], p1, 0, 0, 0); }
}
__device__ __forceinline__ int v_st(int k, int c) { const int kk = (k & ~0xC) | ((k & 4) << 1) | ((k & 8) >> 1); return ((kk >> 3) * 4 + (c >> 5)) * 512 + ((kk & 7) * 32 + (c & 31)) * 2; }
__device__ __forceinline__ int v_rd_base(int lane) { return ((lane & 3) << 3) | (((lane >> 2) & 3) << 6) | (((lane >> 4) & 1) << 5) | (((lane >> 5) & 1) << 8); }
constexpr int v_rd_off(int d0, int ks, int half) { return d0 * 512 + ks * 4096 + half * 2048; }
template <int OFF> __device__ __forceinline__ s16x4 tr_read(int vb) {
  s16x4 r; asm volatile("ds_read_b64_tr_b16 %0, %1 offset:%2" : "=&v"(r) : "v"(vb), "i"(OFF) : "memory"); return r;
}
template <int D0> __device__ __forceinline__ void pv_one(f32x16& od, int vb, bf16x8 pa0, bf16x8 pa1, bf16x8 pa2, bf16x8 pa3) {
  const s16x4 l0 = tr_read<v_rd_off(D0, 0, 0)>(vb), h0 = tr_read<v_rd_off(D0, 0, 1)>(vb), l1 = tr_read<v_rd_off(D0, 1, 0)>(vb), h1 = tr_read<v_rd_off(D0, 1, 1)>(vb);
  const s16x4 l2 = tr_read<v_rd_off(D0, 2, 0)>(vb), h2 = tr_read<v_rd_off(D0, 2, 1)>(vb), l3 = tr_read<v_rd_off(D0, 3, 0)>(vb), h3 = tr_read<v_rd_off(D0, 3, 1)>(vb);
  asm volatile("s_waitcnt lgkmcnt(0)" ::: "memory"); SBAR();
#define PK(L, H) (bf16x8){L[0], L[1], L[2], L[3], H[0], H[1], H[2], H[3]}
  od = __builtin_amdgcn_mfma_f32_32x32x16_bf16(pa0, PK(l0, h0), od, 0, 0, 0);
  od = __builtin_amdgcn_mfma_f32_32x32x16_bf16(pa1, PK(l1, h1), od, 0, 0, 0);
  od = __builtin_amdgcn_mfma_f32_32x32x16_bf16(pa2, PK(l2, h2), od, 0, 0, 0);
  od = __builtin_amdgcn_mfma_f32_32x32x16_bf16(pa3, PK(l3, h3), od, 0, 0, 0);
#undef PK
}
__device__ __forceinline__ void pv_d0(f32x16* o, int vb, bf16x8 pa0, bf16x8 pa1, bf16x8 pa2, bf16x8 pa3) {
  pv_one<0>(o[0], vb, pa0, pa1, pa2, pa3); pv_one<1>(o[1], vb, pa0, pa1, pa2, pa3); pv_one<2>(o[2], vb, pa0, pa1, pa2, pa3); pv_one<3>(o[3], vb, pa0, pa1, pa2, pa3);
}

constexpr size_t SHM_FUSED = 131072;
__global__ __launch_bounds__(512, 1) void k_attn(const u16* __restrict__ X, const u16* __restrict__ Wq, const float* __restrict__ mod,
                                                 const u16* __restrict__ Kc, const u16* __restrict__ Vc, u16* __restrict__ O) {
  extern __shared__ __attribute__((aligned(16))) char lds[];
  const int b = blockIdx.x;
  const int head = (b & 7) + 8 * ((b >> 3) & 3), qb = b >> 5;
  const u16* Kh = Kc + (long)head * S_ * DH;
  const u16* Vh = Vc + (long)head * S_ * DH;
  u16* Ob = O + (long)qb * 256 * HD + head * DH;
  const int tid = threadIdx.x, wid = tid >> 6, lane = tid & 63, r32 = lane & 31, hi = lane >> 5;
  u16* V_lds = (u16*)lds; u16* K_lds = (u16*)(lds + 2 * SHM_V);
  float* ws = (float*)(lds + 2 * SHM_V + 2 * SHM_K) + wid * 64; float* li_l = ws; float* al_l = ws + 32;
  float m_reg = -1e30f, l_reg = 0; f32x16 o[4] = {}; bf16x8 qr[8];
  {
    const unsigned l0 = (unsigned)(uintptr_t)lds + __builtin_amdgcn_readfirstlane(wid) * 1024;
#pragma unroll
    for (int i = 0; i < 16; ++i) {
      const int c = tid + i * 512, panel = c >> 11, row = (c & 2047) >> 2, ch = c & 3;
      glds16(Wq + (long)row * HD + head * DH + panel * 32 + ch * 8, l0 + i * 8192);
    }
    const u16* xr = X + (long)(qb * 256 + wid * QBLK + r32) * D_ + hi * 8;
    bf16x8 xf[32];
#pragma unroll
    for (int ks = 0; ks < 32; ++ks) xf[ks] = *reinterpret_cast<const bf16x8*>(xr + ks * 16);
    float sum = 0.f;
#pragma unroll
    for (int ks = 0; ks < 32; ++ks)
#pragma unroll
      for (int j = 0; j < 8; ++j) sum += bf2f((u16)xf[ks][j]);
    sum += __shfl_xor(sum, 32, 64);
#pragma unroll
    for (int ks = 0; ks < 32; ++ks) asm volatile("" : "+v"(xf[ks]));
    const float mean = sum * (1.f / D_);
    float sq = 0.f;
#pragma unroll
    for (int ks = 0; ks < 32; ++ks)
#pragma unroll
      for (int j = 0; j < 8; ++j) { float d = bf2f((u16)xf[ks][j]) - mean; sq += d * d; }
    sq += __shfl_xor(sq, 32, 64);
#pragma unroll
    for (int ks = 0; ks < 32; ++ks) asm volatile("" : "+v"(xf[ks]));
    const float rstd = rsqrtf(sq * (1.f / D_) + 1e-5f);
    asm volatile("s_waitcnt vmcnt(0)" ::: "memory");
    __syncthreads();
    f32x16 qa[4] = {};
    const char* wb = lds + (8 * hi + ((lane & 15) >> 2)) * 64 + ((lane >> 4) & 1) * 32 + (lane & 3) * 8;
#pragma unroll
    for (int kc = 0; kc < 16; ++kc) {
      SBAR();
#pragma unroll
      for (int k2 = 0; k2 < 2; ++k2) {
        const int ks = kc * 2 + k2;
        const float4* ps = reinterpret_cast<const float4*>(mod + ks * 16 + hi * 8);
        const float4* pc = reinterpret_cast<const float4*>(mod + D_ + ks * 16 + hi * 8);
        const float4 s0 = ps[0], s1 = ps[1], c0 = pc[0], c1 = pc[1];
        const float sa[8] = {s0.x, s0.y, s0.z, s0.w, s1.x, s1.y, s1.z, s1.w}, ca[8] = {c0.x, c0.y, c0.z, c0.w, c1.x, c1.y, c1.z, c1.w};
        bf16x8 hf;
#pragma unroll
        for (int j = 0; j < 8; ++j) hf[j] = (short)f2bf((bf2f((u16)xf[ks][j]) - mean) * rstd * (1.f + ca[j]) + sa[j]);
#pragma unroll
        for (int bl = 0; bl < 4; ++bl) {
          const char* pb = wb + bl * 32768 + ks * 1024;
          s16x4 lo = tr_read_b(pb), hh = tr_read_b(pb + 256);
          bf16x8 af = (bf16x8){lo[0], lo[1], lo[2], lo[3], hh[0], hh[1], hh[2], hh[3]};
          qa[bl] = __builtin_amdgcn_mfma_f32_32x32x16_bf16(af, hf, qa[bl], 0, 0, 0);
        }
      }
    }
    SBAR(); asm volatile("s_nop 15\n\ts_nop 15" ::: "memory"); SBAR();
#define PKQ(P, BASE, OUT) do { unsigned a0 = cvtpk(P[BASE + 0], P[BASE + 1]), a1 = cvtpk(P[BASE + 2], P[BASE + 3]);   \
    unsigned b0 = cvtpk(P[BASE + 4], P[BASE + 5]), b1 = cvtpk(P[BASE + 6], P[BASE + 7]);                              \
    auto r0 = __builtin_amdgcn_permlane32_swap(a0, b0, false, false); auto r1 = __builtin_amdgcn_permlane32_swap(a1, b1, false, false); \
    u32x4 w = {r0[0], r1[0], r0[1], r1[1]}; OUT = *reinterpret_cast<bf16x8*>(&w); } while (0)
#pragma unroll
    for (int bl = 0; bl < 4; ++bl) { PKQ(qa[bl], 0, qr[2 * bl]); PKQ(qa[bl], 8, qr[2 * bl + 1]); }
#undef PKQ
    __syncthreads();
  }
  const int sr = tid >> 4, sc = (tid & 15) * 8, vst0 = v_st(sr, sc), vst1 = v_st(32 + sr, sc);
  const int vb0 = (int)(uintptr_t)V_lds + v_rd_base(lane);
  struct { bf16x8 vs0, vs1, ks0, ks1; } sr_[2];
#define LD8(p) (*reinterpret_cast<const bf16x8*>(p))
#define SLOAD(i, k0) do { sr_[i].vs0 = LD8(&Vh[(long)((k0) + sr) * DH + sc]); sr_[i].vs1 = LD8(&Vh[(long)((k0) + 32 + sr) * DH + sc]); \
    sr_[i].ks0 = LD8(&Kh[(long)((k0) + sr) * DH + sc]); sr_[i].ks1 = LD8(&Kh[(long)((k0) + 32 + sr) * DH + sc]); } while (0)
#define SWRITE(b, i) do { *(bf16x8*)((char*)V_lds + (b) * SHM_V + vst0) = sr_[i].vs0;          \
    *(bf16x8*)((char*)V_lds + (b) * SHM_V + vst1) = sr_[i].vs1; int kc = sc * 2;               \
    *(bf16x8*)((char*)K_lds + (b) * SHM_K + KSWZ(sr, kc)) = sr_[i].ks0;                       \
    *(bf16x8*)((char*)K_lds + (b) * SHM_K + KSWZ(32 + sr, kc)) = sr_[i].ks1; } while (0)
#define SWAIT() asm volatile("s_waitcnt vmcnt(4)" ::: "memory")
#define RESC(a) do { if (__any((a) < 1.f)) { if (hi == 0) al_l[r32] = (a); asm volatile("s_waitcnt lgkmcnt(0)" ::: "memory"); \
    for (int d = 0; d < 4; ++d) for (int r = 0; r < 16; ++r) o[d][r] *= al_l[crow(r, hi)]; } } while (0)
  f32x16 pA0, pA1, pB0, pB1; float mnA, mnB, alA, alB; bf16x8 pa0, pa1, pa2, pa3; constexpr int NT = S_ / KVBLK;
  constexpr int SE = 0, SO = 1;
  SLOAD(SE, 0); asm volatile("s_waitcnt vmcnt(0)" ::: "memory"); SWRITE(0, SE); __syncthreads();
  qkt(pA0, pA1, K_lds, qr, r32, hi); partialSM(pA0, pA1, m_reg, mnA, alA);
  SLOAD(SO, KVBLK); SLOAD(SE, 2 * KVBLK);
  SWAIT(); SWRITE(1, SO); __syncthreads();
  for (int j = 1; j + 1 < NT; j += 2) {
    SBAR(); qkt(pB0, pB1, (u16*)((char*)K_lds + SHM_K), qr, r32, hi);
    finishSM(pA0, pA1, alA, l_reg, pa0, pa1, pa2, pa3); SBAR();
    SLOAD(SO, (j + 2) * KVBLK); SBAR();
    pv_d0(o, vb0, pa0, pa1, pa2, pa3); partialSM(pB0, pB1, m_reg, mnB, alB);
    __syncthreads(); SWAIT(); SWRITE(0, SE);
    RESC(alB); __syncthreads();
    SBAR(); qkt(pA0, pA1, K_lds, qr, r32, hi);
    finishSM(pB0, pB1, alB, l_reg, pa0, pa1, pa2, pa3); SBAR();
    if (j + 3 < NT) SLOAD(SE, (j + 3) * KVBLK); SBAR();
    pv_d0(o, vb0 + (int)SHM_V, pa0, pa1, pa2, pa3); partialSM(pA0, pA1, m_reg, mnA, alA);
    __syncthreads(); SWAIT(); SWRITE(1, SO);
    RESC(alA); __syncthreads();
  }
  SBAR(); qkt(pB0, pB1, (u16*)((char*)K_lds + SHM_K), qr, r32, hi);
  finishSM(pA0, pA1, alA, l_reg, pa0, pa1, pa2, pa3); SBAR();
  pv_d0(o, vb0, pa0, pa1, pa2, pa3); partialSM(pB0, pB1, m_reg, mnB, alB);
  __syncthreads(); RESC(alB);
  finishSM(pB0, pB1, alB, l_reg, pa0, pa1, pa2, pa3); SBAR();
  pv_d0(o, vb0 + (int)SHM_V, pa0, pa1, pa2, pa3);
  if (hi == 0) li_l[r32] = l_reg; asm volatile("s_waitcnt lgkmcnt(0)" ::: "memory");
  float rli[16];
#pragma unroll
  for (int r = 0; r < 16; ++r) rli[r] = __builtin_amdgcn_rcpf(li_l[crow(r, hi)]);
  u16* Ow = Ob + (long)(wid * QBLK) * HD;
#pragma unroll
  for (int r = 0; r < 16; ++r) { int orow = crow(r, hi);
#pragma unroll
    for (int d0 = 0; d0 < 4; ++d0) Ow[(long)orow * HD + d0 * 32 + r32] = f2bf(o[d0][r] * rli[r]); }
#undef SLOAD
#undef SWRITE
#undef SWAIT
#undef RESC
}

extern "C" void kernel_launch(void* const* d_in, const int* in_sizes, int n_in, void* d_out, int out_size, void* d_ws, size_t ws_size,
                              hipStream_t stream) {
  const u16* x = (const u16*)d_in[0];
  const u16* kc = (const u16*)d_in[1];
  const u16* vc = (const u16*)d_in[2];
  const u16* wq = (const u16*)d_in[3];
  const u16* wo = (const u16*)d_in[4];
  const u16* w_in = (const u16*)d_in[6];
  const u16* w_sp = (const u16*)d_in[7];
  const u16* b_sp = (const u16*)d_in[8];
  const u16* w_out = (const u16*)d_in[9];
  const u16* c = (const u16*)d_in[10];
  const u16* w_mod = (const u16*)d_in[11];
  u16* out = (u16*)d_out;

  char* ws = (char*)d_ws;
  size_t off = 0;
  auto carve = [&](size_t bytes) { void* p = ws + off; off += (bytes + 255) / 256 * 256; return p; };
  float* mod = (float*)carve(3072 * 4);
  u16* h1 = (u16*)carve((size_t)S_ * D_ * 2);
  u16* q = (u16*)carve((size_t)S_ * HD * 2);
  u16* o = (u16*)carve((size_t)S_ * HD * 2);
  float* part = (float*)carve((size_t)4 * S_ * D_ * 4);
  u16* x1 = (u16*)carve((size_t)S_ * D_ * 2);
  u16* h2 = (u16*)carve((size_t)S_ * D_ * 2);
  u16* z = (u16*)carve((size_t)S_ * F_ * 2);
  u16* z2n = (u16*)carve((size_t)S_ * FH * 2);
  u16* u = (u16*)carve((size_t)S_ * FH * 2);

  constexpr int GS = 2 * (128 * 64 * 2 + 64 * 128 * 2);
  hipFuncSetAttribute((const void*)k_gemm<64, 128, 2, 2, EPI_BF16, 3>, hipFuncAttributeMaxDynamicSharedMemorySize, 3 * 24576);
  hipFuncSetAttribute((const void*)k_gemm<64, 128, 2, 2, EPI_F32, 3>, hipFuncAttributeMaxDynamicSharedMemorySize, 3 * 24576);
  hipFuncSetAttribute((const void*)k_gemm<64, 128, 2, 2, EPI_GELU, 3>, hipFuncAttributeMaxDynamicSharedMemorySize, 3 * 24576);
  hipFuncSetAttribute((const void*)k_gemm<64, 128, 2, 2, EPI_SP, 4>, hipFuncAttributeMaxDynamicSharedMemorySize, 4 * 24576);
  hipFuncSetAttribute((const void*)k_gemm<64, 64, 2, 2, EPI_OUT, 4>, hipFuncAttributeMaxDynamicSharedMemorySize, 4 * 16384);
  hipFuncSetAttribute((const void*)k_attn, hipFuncAttributeMaxDynamicSharedMemorySize, (int)SHM_FUSED);

  k_mod<<<48, 256, 0, stream>>>(c, w_mod, mod);
  k_attn<<<256, 512, SHM_FUSED, stream>>>(x, wq, mod, kc, vc, o);
  { EpiArgs ea{part, nullptr, nullptr, nullptr, D_, 0};
    k_gemm<64, 128, 2, 2, EPI_F32, 3><<<dim3(D_ / 128, S_ / 64, 4), 256, 3 * 24576, stream>>>(o, wo, ea, HD, D_, HD, HD / 4); }
  k_resid_ln2<4><<<S_ / 4, 256, 0, stream>>>(x, part, mod, x1, h2);
  { EpiArgs ea{z, nullptr, nullptr, nullptr, F_, 0};
    k_gemm<64, 128, 2, 2, EPI_GELU, 3><<<dim3(F_ / 128, S_ / 64, 1), 256, 3 * 24576, stream>>>(h2, w_in, ea, D_, F_, D_, D_); }
  k_ln_z2<<<S_ / 4, 256, 0, stream>>>(z, z2n);
  { EpiArgs ea{u, z, b_sp, nullptr, FH, 0};
    k_gemm<64, 128, 2, 2, EPI_SP, 4><<<dim3(FH / 128, S_ / 64, 1), 256, 4 * 24576, stream>>>(w_sp, z2n, ea, S_, FH, S_, S_); }
  { EpiArgs ea{out, x1, nullptr, mod + 5 * D_, D_, 0};
    k_gemm<64, 64, 2, 2, EPI_OUT, 4><<<dim3(D_ / 64, S_ / 64, 1), 256, 4 * 16384, stream>>>(u, w_out, ea, FH, D_, FH, FH); }
}
```

```cpp
#include <hip/hip_runtime.h>
#include <hip/hip_bf16.h>
#include <stdint.h>

typedef unsigned short u16;
typedef __attribute__((ext_vector_type(8))) short bf16x8;
typedef __attribute__((ext_vector_type(4))) short s16x4;
typedef __attribute__((ext_vector_type(16))) float f32x16;
typedef __attribute__((ext_vector_type(4))) unsigned u32x4;

constexpr int S_ = 2048, D_ = 512, H_ = 32, DH = 128, HD = H_ * DH;
constexpr int F_ = 1792, FH = 896;

__device__ __forceinline__ float bf2f(u16 v) { return __uint_as_float((unsigned)v << 16); }
__device__ __forceinline__ u16 f2bf(float f) {
  unsigned u = __float_as_uint(f);
  u += 0x7fffu + ((u >> 16) & 1u);
  return (u16)(u >> 16);
}
__device__ __forceinline__ float rbf(float f) { return bf2f(f2bf(f)); }
__device__ __forceinline__ unsigned cvtpk(float lo, float hi) {
  unsigned r; asm volatile("v_cvt_pk_bf16_f32 %0, %1, %2" : "=v"(r) : "v"(lo), "v"(hi)); return r;
}
__device__ __forceinline__ int crow(int r, int hi) { return (r & 3) + 8 * (r >> 2) + 4 * hi; }
typedef __attribute__((ext_vector_type(4))) __bf16 bfv4;
typedef __attribute__((address_space(3))) bfv4 lds_bfv4;
__device__ __forceinline__ s16x4 tr_read_b(const void* lds_ptr) {
  bfv4 v = __builtin_amdgcn_ds_read_tr16_b64_v4bf16((lds_bfv4*)(lds_ptr));
  return *reinterpret_cast<s16x4*>(&v);
}
__device__ __forceinline__ float wave_sum(float v) {
#pragma unroll
  for (int o = 32; o >= 1; o >>= 1) v += __shfl_xor(v, o, 64);
  return v;
}

__global__ __launch_bounds__(256) void k_mod(const u16* __restrict__ c, const u16* __restrict__ w_mod, float* __restrict__ mod) {
  __shared__ float red[32][65];
  const int cg = threadIdx.x & 7, kg = threadIdx.x >> 3;
  float acc[8];
#pragma unroll
  for (int j = 0; j < 8; ++j) acc[j] = 0.f;
  bf16x8 wv[16];
#pragma unroll
  for (int k = 0; k < 16; ++k) wv[k] = *reinterpret_cast<const bf16x8*>(w_mod + (long)(kg * 16 + k) * 3072 + blockIdx.x * 64 + cg * 8);
#pragma unroll
  for (int k = 0; k < 16; ++k) {
    float cv = bf2f(c[kg * 16 + k]);
    float sv = rbf(cv / (1.f + __expf(-cv)));
#pragma unroll
    for (int j = 0; j < 8; ++j) acc[j] += sv * bf2f((u16)wv[k][j]);
  }
#pragma unroll
  for (int j = 0; j < 8; ++j) red[kg][cg * 8 + j] = acc[j];
  __syncthreads();
  if (threadIdx.x < 64) {
    float t = 0.f;
#pragma unroll
    for (int g = 0; g < 32; ++g) t += red[g][threadIdx.x];
    mod[blockIdx.x * 64 + threadIdx.x] = rbf(t);
  }
}

__global__ __launch_bounds__(256) void k_ln1(const u16* __restrict__ x, const float* __restrict__ mod, u16* __restrict__ h) {
  const int lane = threadIdx.x & 63, row = blockIdx.x * 4 + (threadIdx.x >> 6);
  bf16x8 xv = *reinterpret_cast<const bf16x8*>(x + (long)row * D_ + lane * 8);
  float v[8]; float s = 0.f;
#pragma unroll
  for (int j = 0; j < 8; ++j) { v[j] = bf2f((u16)xv[j]); s += v[j]; }
  float mean = wave_sum(s) * (1.f / D_);
  float q = 0.f;
#pragma unroll
  for (int j = 0; j < 8; ++j) { float d = v[j] - mean; q += d * d; }
  float rstd = rsqrtf(wave_sum(q) * (1.f / D_) + 1e-5f);
  bf16x8 o;
#pragma unroll
  for (int j = 0; j < 8; ++j) {
    int cidx = lane * 8 + j;
    o[j] = (short)f2bf((v[j] - mean) * rstd * (1.f + mod[D_ + cidx]) + mod[cidx]);
  }
  *reinterpret_cast<bf16x8*>(h + (long)row * D_ + lane * 8) = o;
}

enum { EPI_BF16 = 0, EPI_F32 = 1, EPI_GELU = 2, EPI_SP = 3, EPI_OUT = 4 };
struct EpiArgs {
  void* C;
  const u16* aux0;
  const u16* aux1;
  const float* auxf;
  int ldc;
  int pad;
};

__device__ __forceinline__ float gelu_tanh(float x) {
  const float k0 = 0.7978845608028654f, k1 = 0.044715f;
  float u = k0 * (x + k1 * x * x * x);
  float e = __expf(2.f * u);
  float t = 1.f - 2.f / (e + 1.f);
  return 0.5f * x * (1.f + t);
}

template <int N> __device__ __forceinline__ void wait_vm() { asm volatile("s_waitcnt vmcnt(%0)" :: "n"(N) : "memory"); }
__device__ __forceinline__ void glds16(const void* gsrc, unsigned lds_dst) {
  unsigned keep;
  asm volatile("s_mov_b32 %0, m0\n\ts_mov_b32 m0, %2\n\ts_nop 0\n\tglobal_load_lds_dwordx4 %1, off\n\ts_mov_b32 m0, %0"
               : "=&s"(keep) : "v"(gsrc), "s"(lds_dst) : "memory");
}
template <int BM, int BN, int WM, int WN, int EPI, int NST>
__global__ __launch_bounds__(WM * WN * 64) void k_gemm(const u16* __restrict__ A, const u16* __restrict__ B, EpiArgs ea,
                                                      int lda, int ldb, int K, int ksplit) {
  constexpr int BK = 64, NT = WM * WN * 64;
  constexpr int TM = BM / WM / 32, TN = BN / WN / 32;
  constexpr int ACH = BM * 8 / NT, BCH = BN * 8 / NT, G = ACH + BCH;
  constexpr int ABYTES = BM * BK * 2, BBYTES = BK * BN * 2, STB = ABYTES + BBYTES;
  extern __shared__ __attribute__((aligned(16))) char smem[];
  const int tid = threadIdx.x, lane = tid & 63, wid = tid >> 6;
  const int wm = wid / WN, wn = wid % WN;
  const int r32 = lane & 31, hi = lane >> 5;
  const int bm = blockIdx.y * BM, bn = blockIdx.x * BN;
  const int kbeg = blockIdx.z * ksplit;
  const int nt = ksplit / BK;
  A += (long)bm * lda + kbeg;
  B += (long)kbeg * ldb + bn;
  const unsigned lds0 = (unsigned)(uintptr_t)smem;
  const unsigned wid_u = __builtin_amdgcn_readfirstlane(wid);
  auto stage = [&](int t, int buf) {
    const unsigned sa = lds0 + buf * STB + wid_u * 1024;
    const unsigned sb = sa + ABYTES;
#pragma unroll
    for (int i = 0; i < ACH; ++i) {
      int c = tid + i * NT; int row = c >> 3, ch = (c & 7) ^ ((row >> 1) & 7);
      glds16(A + (long)row * lda + t * BK + ch * 8, sa + i * NT * 16);
    }
#pragma unroll
    for (int i = 0; i < BCH; ++i) {
      int c = tid + i * NT; int panel = c >> 8, row = (c & 255) >> 2, ch = c & 3;
      glds16(B + (long)(t * BK + row) * ldb + panel * 32 + ch * 8, sb + i * NT * 16);
    }
  };
  f32x16 acc[TM][TN];
#pragma unroll
  for (int m = 0; m < TM; ++m)
#pragma unroll
    for (int n = 0; n < TN; ++n) acc[m][n] = f32x16{};
#pragma unroll
  for (int s0 = 0; s0 < NST - 1; ++s0) stage(s0, s0);
  const int q4 = (lane & 15) >> 2, p4 = lane & 3, blk = (lane >> 4) & 1;
  int buf = 0, nbuf = NST - 1;
  for (int t = 0; t < nt; ++t) {
    if (t + NST - 2 < nt) wait_vm<(NST - 2) * G>(); else wait_vm<0>();
    __builtin_amdgcn_s_barrier();
    if (t + NST - 1 < nt) stage(t + NST - 1, nbuf);
    const char* sa = smem + buf * STB;
    const char* sb = sa + ABYTES;
#pragma unroll
    for (int s = 0; s < 4; ++s) {
      bf16x8 af[TM], bfr[TN];
#pragma unroll
      for (int m = 0; m < TM; ++m) {
        int row = (wm * TM + m) * 32 + r32; int ch = 2 * s + hi;
        af[m] = *reinterpret_cast<const bf16x8*>(sa + row * 128 + ((ch ^ ((row >> 1) & 7)) << 4));
      }
#pragma unroll
      for (int n = 0; n < TN; ++n) {
        const char* pb = sb + (wn * TN + n) * 4096 + (16 * s + 8 * hi + q4) * 64 + blk * 32 + p4 * 8;
        s16x4 lo = tr_read_b(pb), hh = tr_read_b(pb + 256);
        bfr[n] = (bf16x8){lo[0], lo[1], lo[2], lo[3], hh[0], hh[1], hh[2], hh[3]};
      }
#pragma unroll
      for (int m = 0; m < TM; ++m)
#pragma unroll
        for (int n = 0; n < TN; ++n)
          acc[m][n] = __builtin_amdgcn_mfma_f32_32x32x16_bf16(af[m], bfr[n], acc[m][n], 0, 0, 0);
    }
    asm volatile("s_waitcnt lgkmcnt(0)" ::: "memory");
    buf = (buf + 1 == NST) ? 0 : buf + 1;
    nbuf = (nbuf + 1 == NST) ? 0 : nbuf + 1;
  }
  constexpr int CLD = BN + 4;
  float* cs = reinterpret_cast<float*>(smem);
  __builtin_amdgcn_s_barrier();
#pragma unroll
  for (int m = 0; m < TM; ++m)
#pragma unroll
    for (int n = 0; n < TN; ++n)
#pragma unroll
      for (int r = 0; r < 16; ++r)
        cs[((wm * TM + m) * 32 + crow(r, hi)) * CLD + (wn * TN + n) * 32 + r32] = acc[m][n][r];
  __syncthreads();
  constexpr int NCH = BM * BN / 8 / NT;
  bf16x8 a0[NCH]; float bsp[NCH];
#pragma unroll
  for (int i = 0; i < NCH; ++i) {
    const int c = tid + i * NT, row = bm + c / (BN / 8), col = bn + (c % (BN / 8)) * 8;
    if constexpr (EPI == EPI_SP) { a0[i] = *reinterpret_cast<const bf16x8*>(ea.aux0 + (long)row * F_ + col); bsp[i] = bf2f(ea.aux1[row]); }
    if constexpr (EPI == EPI_OUT) a0[i] = *reinterpret_cast<const bf16x8*>(ea.aux0 + (long)row * D_ + col);
  }
#pragma unroll
  for (int i = 0; i < NCH; ++i) {
    const int c = tid + i * NT, lr = c / (BN / 8), lc = (c % (BN / 8)) * 8, row = bm + lr, col = bn + lc;
    const float4 u0 = *reinterpret_cast<const float4*>(cs + lr * CLD + lc), u1 = *reinterpret_cast<const float4*>(cs + lr * CLD + lc + 4);
    float v[8] = {u0.x, u0.y, u0.z, u0.w, u1.x, u1.y, u1.z, u1.w};
    if constexpr (EPI == EPI_F32) {
      float* dst = (float*)ea.C + ((long)blockIdx.z * S_ + row) * ea.ldc + col;
      *reinterpret_cast<float4*>(dst) = u0; *reinterpret_cast<float4*>(dst + 4) = u1;
    } else {
      bf16x8 o;
#pragma unroll
      for (int j = 0; j < 8; ++j) {
        float y;
        if constexpr (EPI == EPI_BF16) y = v[j];
        else if constexpr (EPI == EPI_GELU) y = gelu_tanh(rbf(v[j]));
        else if constexpr (EPI == EPI_SP) y = bf2f((u16)a0[i][j]) * rbf(rbf(v[j]) + bsp[i]);
        else y = bf2f((u16)a0[i][j]) + rbf(ea.auxf[col + j] * rbf(v[j]));
        o[j] = (short)f2bf(y);
      }
      *reinterpret_cast<bf16x8*>((u16*)ea.C + (long)row * ea.ldc + col) = o;
    }
  }
}

template <int NSPLIT>
__global__ __launch_bounds__(256) void k_resid_ln2(const u16* __restrict__ x, const float* __restrict__ part, const float* __restrict__ mod,
                                                   u16* __restrict__ x1o, u16* __restrict__ h2) {
  const int lane = threadIdx.x & 63, row = blockIdx.x * 4 + (threadIdx.x >> 6);
  bf16x8 xv = *reinterpret_cast<const bf16x8*>(x + (long)row * D_ + lane * 8);
  float a[8];
#pragma unroll
  for (int j = 0; j < 8; ++j) a[j] = 0.f;
#pragma unroll
  for (int p = 0; p < NSPLIT; ++p) {
    const float4* pp = reinterpret_cast<const float4*>(part + ((long)p * S_ + row) * D_ + lane * 8);
    float4 u0 = pp[0], u1 = pp[1];
    a[0] += u0.x; a[1] += u0.y; a[2] += u0.z; a[3] += u0.w; a[4] += u1.x; a[5] += u1.y; a[6] += u1.z; a[7] += u1.w;
  }
  float v[8]; float s = 0.f; bf16x8 xo;
#pragma unroll
  for (int j = 0; j < 8; ++j) {
    int cidx = lane * 8 + j;
    float t = rbf(bf2f((u16)xv[j]) + rbf(mod[2 * D_ + cidx] * rbf(a[j])));
    v[j] = t; s += t; xo[j] = (short)f2bf(t);
  }
  *reinterpret_cast<bf16x8*>(x1o + (long)row * D_ + lane * 8) = xo;
  float mean = wave_sum(s) * (1.f / D_);
  float q = 0.f;
#pragma unroll
  for (int j = 0; j < 8; ++j) { float d = v[j] - mean; q += d * d; }
  float rstd = rsqrtf(wave_sum(q) * (1.f / D_) + 1e-5f);
  bf16x8 o;
#pragma unroll
  for (int j = 0; j < 8; ++j) {
    int cidx = lane * 8 + j;
    o[j] = (short)f2bf((v[j] - mean) * rstd * (1.f + mod[4 * D_ + cidx]) + mod[3 * D_ + cidx]);
  }
  *reinterpret_cast<bf16x8*>(h2 + (long)row * D_ + lane * 8) = o;
}

__global__ __launch_bounds__(256) void k_ln_z2(const u16* __restrict__ z, u16* __restrict__ z2n) {
  const int lane = threadIdx.x & 63, row = blockIdx.x * 4 + (threadIdx.x >> 6);
  float v[16]; float s = 0.f; const bool act = lane < 56;
  bf16x8 c0 = {}, c1 = {};
  if (act) {
    c0 = *reinterpret_cast<const bf16x8*>(z + (long)row * F_ + FH + lane * 16);
    c1 = *reinterpret_cast<const bf16x8*>(z + (long)row * F_ + FH + lane * 16 + 8);
  }
#pragma unroll
  for (int j = 0; j < 8; ++j) { v[j] = bf2f((u16)c0[j]); v[8 + j] = bf2f((u16)c1[j]); s += v[j] + v[8 + j]; }
  float mean = wave_sum(s) * (1.f / FH);
  float q = 0.f;
  if (act) {
#pragma unroll
    for (int j = 0; j < 16; ++j) { float d = v[j] - mean; q += d * d; }
  }
  float rstd = rsqrtf(wave_sum(q) * (1.f / FH) + 1e-5f);
  if (act) {
    bf16x8 o0, o1;
#pragma unroll
    for (int j = 0; j < 8; ++j) { o0[j] = (short)f2bf((v[j] - mean) * rstd); o1[j] = (short)f2bf((v[8 + j] - mean) * rstd); }
    *reinterpret_cast<bf16x8*>(z2n + (long)row * FH + lane * 16) = o0;
    *reinterpret_cast<bf16x8*>(z2n + (long)row * FH + lane * 16 + 8) = o1;
  }
}

constexpr int NW = 8, QBLK = 32, KVBLK = 64;
constexpr float SCALE = 0.088388347648318440f;
constexpr float THR = 8.f;
constexpr size_t SHM_V = KVBLK * DH * 2, SHM_K = KVBLK * DH * 2, SHM_ATTN = 2 * SHM_V + 2 * SHM_K + NW * 64 * 4;
#define KSWZ(row, colB) ((row) * 256 + ((colB) ^ (((row) & 7) << 4)))
#define SBAR() __builtin_amdgcn_sched_barrier(0)

__device__ __forceinline__ void partialSM(f32x16& p0, f32x16& p1, float& m_reg, float& mn, float& alpha) {
  constexpr float C = SCALE * 1.4426950408889634f;
  float pmax = p0[0];
  for (int r = 1; r < 16; ++r) pmax = fmaxf(pmax, p0[r]);
  for (int r = 0; r < 16; ++r) pmax = fmaxf(pmax, p1[r]);
  { auto rr = __builtin_amdgcn_permlane32_swap(__float_as_uint(pmax), __float_as_uint(pmax), false, false);
    pmax = fmaxf(__uint_as_float(rr[0]), __uint_as_float(rr[1])); }
  if (__builtin_expect(__all(pmax - m_reg <= THR / SCALE), 1)) { mn = m_reg; alpha = 1.f; }
  else { mn = fmaxf(m_reg, pmax); alpha = __builtin_amdgcn_exp2f((m_reg - mn) * C); m_reg = mn; }
  float mnC = -mn * C;
  for (int r = 0; r < 16; ++r) p0[r] = fmaf(p0[r], C, mnC);
  for (int r = 0; r < 16; ++r) p1[r] = fmaf(p1[r], C, mnC);
  for (int r = 0; r < 16; ++r) p0[r] = __builtin_amdgcn_exp2f(p0[r]);
}
__device__ __forceinline__ void finishSM(f32x16& p0, f32x16& p1, float alpha, float& l_reg, bf16x8& pa0, bf16x8& pa1, bf16x8& pa2, bf16x8& pa3) {
  for (int r = 0; r < 16; ++r) p1[r] = __builtin_amdgcn_exp2f(p1[r]);
  float ps = 0;
  for (int r = 0; r < 16; ++r) ps += p0[r];
  for (int r = 0; r < 16; ++r) ps += p1[r];
  { auto rr = __builtin_amdgcn_permlane32_swap(__float_as_uint(ps), __float_as_uint(ps), false, false);
    ps = __uint_as_float(rr[0]) + __uint_as_float(rr[1]); }
  l_reg = l_reg * alpha + ps;
#define PK4(P, BASE, OUT) do { unsigned a0 = cvtpk(P[BASE + 0], P[BASE + 1]), a1 = cvtpk(P[BASE + 2], P[BASE + 3]);   \
    unsigned b0 = cvtpk(P[BASE + 4], P[BASE + 5]), b1 = cvtpk(P[BASE + 6], P[BASE + 7]);                              \
    auto r0 = __builtin_amdgcn_permlane32_swap(a0, b0, false, false); auto r1 = __builtin_amdgcn_permlane32_swap(a1, b1, false, false); \
    u32x4 w = {r0[0], r1[0], r0[1], r1[1]}; OUT = *reinterpret_cast<bf16x8*>(&w); } while (0)
  PK4(p0, 0, pa0); PK4(p0, 8, pa1); PK4(p1, 0, pa2); PK4(p1, 8, pa3);
#undef PK4
}
__device__ __forceinline__ void qkt(f32x16& p0, f32x16& p1, const u16* Ks, const bf16x8* qr, int r32, int hi) {
  p0 = f32x16{}; p1 = f32x16{};
  for (int d0 = 0; d0 < 8; ++d0) { int cb = (d0 * 16 + hi * 8) * 2;
    bf16x8 b0 = *reinterpret_cast<const bf16x8*>((const char*)Ks + KSWZ(r32, cb));
    bf16x8 b1 = *reinterpret_cast<const bf16x8*>((const char*)Ks + KSWZ(32 + r32, cb));
    p0 = __builtin_amdgcn_mfma_f32_32x32x16_bf16(b0, qr[d0], p0, 0, 0, 0);
    p1 = __builtin_amdgcn_mfma_f32_32x32x16_bf16(b1, qr[d0], p1, 0, 0, 0); }
}
__device__ __forceinline__ int v_st(int k, int c) { const int kk = (k & ~0xC) | ((k & 4) << 1) | ((k & 8) >> 1); return ((kk >> 3) * 4 + (c >> 5)) * 512 + ((kk & 7) * 32 + (c & 31)) * 2; }
__device__ __forceinline__ int v_rd_base(int lane) { return ((lane & 3) << 3) | (((lane >> 2) & 3) << 6) | (((lane >> 4) & 1) << 5) | (((lane >> 5) & 1) << 8); }
constexpr int v_rd_off(int d0, int ks, int half) { return d0 * 512 + ks * 4096 + half * 2048; }
template <int OFF> __device__ __forceinline__ s16x4 tr_read(int vb) {
  s16x4 r; asm volatile("ds_read_b64_tr_b16 %0, %1 offset:%2" : "=&v"(r) : "v"(vb), "i"(OFF) : "memory"); return r;
}
template <int D0> __device__ __forceinline__ void pv_one(f32x16& od, int vb, bf16x8 pa0, bf16x8 pa1, bf16x8 pa2, bf16x8 pa3) {
  const s16x4 l0 = tr_read<v_rd_off(D0, 0, 0)>(vb), h0 = tr_read<v_rd_off(D0, 0, 1)>(vb), l1 = tr_read<v_rd_off(D0, 1, 0)>(vb), h1 = tr_read<v_rd_off(D0, 1, 1)>(vb);
  const s16x4 l2 = tr_read<v_rd_off(D0, 2, 0)>(vb), h2 = tr_read<v_rd_off(D0, 2, 1)>(vb), l3 = tr_read<v_rd_off(D0, 3, 0)>(vb), h3 = tr_read<v_rd_off(D0, 3, 1)>(vb);
  asm volatile("s_waitcnt lgkmcnt(0)" ::: "memory"); SBAR();
#define PK(L, H) (bf16x8){L[0], L[1], L[2], L[3], H[0], H[1], H[2], H[3]}
  od = __builtin_amdgcn_mfma_f32_32x32x16_bf16(pa0, PK(l0, h0), od, 0, 0, 0);
  od = __builtin_amdgcn_mfma_f32_32x32x16_bf16(pa1, PK(l1, h1), od, 0, 0, 0);
  od = __builtin_amdgcn_mfma_f32_32x32x16_bf16(pa2, PK(l2, h2), od, 0, 0, 0);
  od = __builtin_amdgcn_mfma_f32_32x32x16_bf16(pa3, PK(l3, h3), od, 0, 0, 0);
#undef PK
}
__device__ __forceinline__ void pv_d0(f32x16* o, int vb, bf16x8 pa0, bf16x8 pa1, bf16x8 pa2, bf16x8 pa3) {
  pv_one<0>(o[0], vb, pa0, pa1, pa2, pa3); pv_one<1>(o[1], vb, pa0, pa1, pa2, pa3); pv_one<2>(o[2], vb, pa0, pa1, pa2, pa3); pv_one<3>(o[3], vb, pa0, pa1, pa2, pa3);
}

constexpr size_t SHM_FUSED = 131072 + 4096;
__global__ __launch_bounds__(512, 1) void k_attn(const u16* __restrict__ X, const u16* __restrict__ Wq, const float* __restrict__ mod,
                                                 const u16* __restrict__ Kc, const u16* __restrict__ Vc, u16* __restrict__ O) {
  extern __shared__ __attribute__((aligned(16))) char lds[];
  const int b = blockIdx.x;
  const int head = (b & 7) + 8 * ((b >> 3) & 3), qb = b >> 5;
  const u16* Kh = Kc + (long)head * S_ * DH;
  const u16* Vh = Vc + (long)head * S_ * DH;
  u16* Ob = O + (long)qb * 256 * HD + head * DH;
  const int tid = threadIdx.x, wid = tid >> 6, lane = tid & 63, r32 = lane & 31, hi = lane >> 5;
  u16* V_lds = (u16*)lds; u16* K_lds = (u16*)(lds + 2 * SHM_V);
  float* ws = (float*)(lds + 2 * SHM_V + 2 * SHM_K) + wid * 64; float* li_l = ws; float* al_l = ws + 32;
  float m_reg = -1e30f, l_reg = 0; f32x16 o[4] = {}; bf16x8 qr[8];
  {
    const unsigned l0 = (unsigned)(uintptr_t)lds + __builtin_amdgcn_readfirstlane(wid) * 1024;
#pragma unroll
    for (int i = 0; i < 16; ++i) {
      const int c = tid + i * 512, panel = c >> 11, row = (c & 2047) >> 2, ch = c & 3;
      glds16(Wq + (long)row * HD + head * DH + panel * 32 + ch * 8, l0 + i * 8192);
    }
    const u16* xr = X + (long)(qb * 256 + wid * QBLK + r32) * D_ + hi * 64;
    bf16x8 xf[32];
#pragma unroll
    for (int ks = 0; ks < 32; ++ks) xf[ks] = *reinterpret_cast<const bf16x8*>(xr + (ks >> 3) * 128 + (ks & 7) * 8);
    float* ml = reinterpret_cast<float*>(lds + 131072);
    ml[tid] = mod[tid]; ml[512 + tid] = 1.f + mod[512 + tid];
    float sum = 0.f;
#pragma unroll
    for (int ks = 0; ks < 32; ++ks)
#pragma unroll
      for (int j = 0; j < 8; ++j) sum += bf2f((u16)xf[ks][j]);
    sum += __shfl_xor(sum, 32, 64);
#pragma unroll
    for (int ks = 0; ks < 32; ++ks) asm volatile("" : "+v"(xf[ks]));
    const float mean = sum * (1.f / D_);
    float sq = 0.f;
#pragma unroll
    for (int ks = 0; ks < 32; ++ks)
#pragma unroll
      for (int j = 0; j < 8; ++j) { float d = bf2f((u16)xf[ks][j]) - mean; sq += d * d; }
    sq += __shfl_xor(sq, 32, 64);
#pragma unroll
    for (int ks = 0; ks < 32; ++ks) asm volatile("" : "+v"(xf[ks]));
    const float rstd = rsqrtf(sq * (1.f / D_) + 1e-5f);
    asm volatile("s_waitcnt vmcnt(0)" ::: "memory");
    __syncthreads();
    f32x16 qa[4] = {};
    const char* wb = lds + (64 * hi + ((lane & 15) >> 2)) * 64 + ((lane >> 4) & 1) * 32 + (lane & 3) * 8;
#pragma unroll
    for (int kc = 0; kc < 16; ++kc) {
      SBAR();
#pragma unroll
      for (int k2 = 0; k2 < 2; ++k2) {
        const int ks = kc * 2 + k2;
        const int k0 = (ks >> 3) * 128 + (ks & 7) * 8;
        const float4* ps = reinterpret_cast<const float4*>(ml + k0 + hi * 64);
        const float4* pc = reinterpret_cast<const float4*>(ml + 512 + k0 + hi * 64);
        const float4 s0 = ps[0], s1 = ps[1], c0 = pc[0], c1 = pc[1];
        const float sa[8] = {s0.x, s0.y, s0.z, s0.w, s1.x, s1.y, s1.z, s1.w}, ca[8] = {c0.x, c0.y, c0.z, c0.w, c1.x, c1.y, c1.z, c1.w};
        bf16x8 hf;
#pragma unroll
        for (int j = 0; j < 8; ++j) hf[j] = (short)f2bf((bf2f((u16)xf[ks][j]) - mean) * rstd * ca[j] + sa[j]);
#pragma unroll
        for (int bl = 0; bl < 4; ++bl) {
          const char* pb = wb + bl * 32768 + k0 * 64;
          s16x4 lo = tr_read_b(pb), hh = tr_read_b(pb + 256);
          bf16x8 af = (bf16x8){lo[0], lo[1], lo[2], lo[3], hh[0], hh[1], hh[2], hh[3]};
          qa[bl] = __builtin_amdgcn_mfma_f32_32x32x16_bf16(af, hf, qa[bl], 0, 0, 0);
        }
      }
    }
    SBAR(); asm volatile("s_nop 15\n\ts_nop 15" ::: "memory"); SBAR();
#define PKQ(P, BASE, OUT) do { unsigned a0 = cvtpk(P[BASE + 0], P[BASE + 1]), a1 = cvtpk(P[BASE + 2], P[BASE + 3]);   \
    unsigned b0 = cvtpk(P[BASE + 4], P[BASE + 5]), b1 = cvtpk(P[BASE + 6], P[BASE + 7]);                              \
    auto r0 = __builtin_amdgcn_permlane32_swap(a0, b0, false, false); auto r1 = __builtin_amdgcn_permlane32_swap(a1, b1, false, false); \
    u32x4 w = {r0[0], r1[0], r0[1], r1[1]}; OUT = *reinterpret_cast<bf16x8*>(&w); } while (0)
#pragma unroll
    for (int bl = 0; bl < 4; ++bl) { PKQ(qa[bl], 0, qr[2 * bl]); PKQ(qa[bl], 8, qr[2 * bl + 1]); }
#undef PKQ
    __syncthreads();
  }
  const int sr = tid >> 4, sc = (tid & 15) * 8, vst0 = v_st(sr, sc), vst1 = v_st(32 + sr, sc);
  const int vb0 = (int)(uintptr_t)V_lds + v_rd_base(lane);
  struct { bf16x8 vs0, vs1, ks0, ks1; } sr_[2];
#define LD8(p) (*reinterpret_cast<const bf16x8*>(p))
#define SLOAD(i, k0) do { sr_[i].vs0 = LD8(&Vh[(long)((k0) + sr) * DH + sc]); sr_[i].vs1 = LD8(&Vh[(long)((k0) + 32 + sr) * DH + sc]); \
    sr_[i].ks0 = LD8(&Kh[(long)((k0) + sr) * DH + sc]); sr_[i].ks1 = LD8(&Kh[(long)((k0) + 32 + sr) * DH + sc]); } while (0)
#define SWRITE(b, i) do { *(bf16x8*)((char*)V_lds + (b) * SHM_V + vst0) = sr_[i].vs0;          \
    *(bf16x8*)((char*)V_lds + (b) * SHM_V + vst1) = sr_[i].vs1; int kc = sc * 2;               \
    *(bf16x8*)((char*)K_lds + (b) * SHM_K + KSWZ(sr, kc)) = sr_[i].ks0;                       \
    *(bf16x8*)((char*)K_lds + (b) * SHM_K + KSWZ(32 + sr, kc)) = sr_[i].ks1; } while (0)
#define SWAIT() asm volatile("s_waitcnt vmcnt(4)" ::: "memory")
#define RESC(a) do { if (__any((a) < 1.f)) { if (hi == 0) al_l[r32] = (a); asm volatile("s_waitcnt lgkmcnt(0)" ::: "memory"); \
    for (int d = 0; d < 4; ++d) for (int r = 0; r < 16; ++r) o[d][r] *= al_l[crow(r, hi)]; } } while (0)
  f32x16 pA0, pA1, pB0, pB1; float mnA, mnB, alA, alB; bf16x8 pa0, pa1, pa2, pa3; constexpr int NT = S_ / KVBLK;
  constexpr int SE = 0, SO = 1;
  SLOAD(SE, 0); asm volatile("s_waitcnt vmcnt(0)" ::: "memory"); SWRITE(0, SE); __syncthreads();
  qkt(pA0, pA1, K_lds, qr, r32, hi); partialSM(pA0, pA1, m_reg, mnA, alA);
  SLOAD(SO, KVBLK); SLOAD(SE, 2 * KVBLK);
  SWAIT(); SWRITE(1, SO); __syncthreads();
  for (int j = 1; j + 1 < NT; j += 2) {
    SBAR(); qkt(pB0, pB1, (u16*)((char*)K_lds + SHM_K), qr, r32, hi);
    finishSM(pA0, pA1, alA, l_reg, pa0, pa1, pa2, pa3); SBAR();
    SLOAD(SO, (j + 2) * KVBLK); SBAR();
    pv_d0(o, vb0, pa0, pa1, pa2, pa3); partialSM(pB0, pB1, m_reg, mnB, alB);
    __syncthreads(); SWAIT(); SWRITE(0, SE);
    RESC(alB); __syncthreads();
    SBAR(); qkt(pA0, pA1, K_lds, qr, r32, hi);
    finishSM(pB0, pB1, alB, l_reg, pa0, pa1, pa2, pa3); SBAR();
    if (j + 3 < NT) SLOAD(SE, (j + 3) * KVBLK); SBAR();
    pv_d0(o, vb0 + (int)SHM_V, pa0, pa1, pa2, pa3); partialSM(pA0, pA1, m_reg, mnA, alA);
    __syncthreads(); SWAIT(); SWRITE(1, SO);
    RESC(alA); __syncthreads();
  }
  SBAR(); qkt(pB0, pB1, (u16*)((char*)K_lds + SHM_K), qr, r32, hi);
  finishSM(pA0, pA1, alA, l_reg, pa0, pa1, pa2, pa3); SBAR();
  pv_d0(o, vb0, pa0, pa1, pa2, pa3); partialSM(pB0, pB1, m_reg, mnB, alB);
  __syncthreads(); RESC(alB);
  finishSM(pB0, pB1, alB, l_reg, pa0, pa1, pa2, pa3); SBAR();
  pv_d0(o, vb0 + (int)SHM_V, pa0, pa1, pa2, pa3);
  if (hi == 0) li_l[r32] = l_reg; asm volatile("s_waitcnt lgkmcnt(0)" ::: "memory");
  float rli[16];
#pragma unroll
  for (int r = 0; r < 16; ++r) rli[r] = __builtin_amdgcn_rcpf(li_l[crow(r, hi)]);
  u16* Ow = Ob + (long)(wid * QBLK) * HD;
#pragma unroll
  for (int r = 0; r < 16; ++r) { int orow = crow(r, hi);
#pragma unroll
    for (int d0 = 0; d0 < 4; ++d0) Ow[(long)orow * HD + d0 * 32 + r32] = f2bf(o[d0][r] * rli[r]); }
#undef SLOAD
#undef SWRITE
#undef SWAIT
#undef RESC
}

extern "C" void kernel_launch(void* const* d_in, const int* in_sizes, int n_in, void* d_out, int out_size, void* d_ws, size_t ws_size,
                              hipStream_t stream) {
  const u16* x = (const u16*)d_in[0];
  const u16* kc = (const u16*)d_in[1];
  const u16* vc = (const u16*)d_in[2];
  const u16* wq = (const u16*)d_in[3];
  const u16* wo = (const u16*)d_in[4];
  const u16* w_in = (const u16*)d_in[6];
  const u16* w_sp = (const u16*)d_in[7];
  const u16* b_sp = (const u16*)d_in[8];
  const u16* w_out = (const u16*)d_in[9];
  const u16* c = (const u16*)d_in[10];
  const u16* w_mod = (const u16*)d_in[11];
  u16* out = (u16*)d_out;

  char* ws = (char*)d_ws;
  size_t off = 0;
  auto carve = [&](size_t bytes) { void* p = ws + off; off += (bytes + 255) / 256 * 256; return p; };
  float* mod = (float*)carve(3072 * 4);
  u16* h1 = (u16*)carve((size_t)S_ * D_ * 2);
  u16* q = (u16*)carve((size_t)S_ * HD * 2);
  u16* o = (u16*)carve((size_t)S_ * HD * 2);
  float* part = (float*)carve((size_t)4 * S_ * D_ * 4);
  u16* x1 = (u16*)carve((size_t)S_ * D_ * 2);
  u16* h2 = (u16*)carve((size_t)S_ * D_ * 2);
  u16* z = (u16*)carve((size_t)S_ * F_ * 2);
  u16* z2n = (u16*)carve((size_t)S_ * FH * 2);
  u16* u = (u16*)carve((size_t)S_ * FH * 2);

  constexpr int GS = 2 * (128 * 64 * 2 + 64 * 128 * 2);
  hipFuncSetAttribute((const void*)k_gemm<64, 128, 2, 2, EPI_BF16, 3>, hipFuncAttributeMaxDynamicSharedMemorySize, 3 * 24576);
  hipFuncSetAttribute((const void*)k_gemm<64, 128, 2, 2, EPI_F32, 3>, hipFuncAttributeMaxDynamicSharedMemorySize, 3 * 24576);
  hipFuncSetAttribute((const void*)k_gemm<64, 128, 2, 2, EPI_GELU, 3>, hipFuncAttributeMaxDynamicSharedMemorySize, 3 * 24576);
  hipFuncSetAttribute((const void*)k_gemm<64, 128, 2, 2, EPI_SP, 4>, hipFuncAttributeMaxDynamicSharedMemorySize, 4 * 24576);
  hipFuncSetAttribute((const void*)k_gemm<64, 64, 2, 2, EPI_OUT, 4>, hipFuncAttributeMaxDynamicSharedMemorySize, 4 * 16384);
  hipFuncSetAttribute((const void*)k_attn, hipFuncAttributeMaxDynamicSharedMemorySize, (int)SHM_FUSED);

  k_mod<<<48, 256, 0, stream>>>(c, w_mod, mod);
  k_attn<<<256, 512, SHM_FUSED, stream>>>(x, wq, mod, kc, vc, o);
  { EpiArgs ea{part, nullptr, nullptr, nullptr, D_, 0};
    k_gemm<64, 128, 2, 2, EPI_F32, 3><<<dim3(D_ / 128, S_ / 64, 4), 256, 3 * 24576, stream>>>(o, wo, ea, HD, D_, HD, HD / 4); }
  k_resid_ln2<4><<<S_ / 4, 256, 0, stream>>>(x, part, mod, x1, h2);
  { EpiArgs ea{z, nullptr, nullptr, nullptr, F_, 0};
    k_gemm<64, 128, 2, 2, EPI_GELU, 3><<<dim3(F_ / 128, S_ / 64, 1), 256, 3 * 24576, stream>>>(h2, w_in, ea, D_, F_, D_, D_); }
  k_ln_z2<<<S_ / 4, 256, 0, stream>>>(z, z2n);
  { EpiArgs ea{u, z, b_sp, nullptr, FH, 0};
    k_gemm<64, 128, 2, 2, EPI_SP, 4><<<dim3(FH / 128, S_ / 64, 1), 256, 4 * 24576, stream>>>(w_sp, z2n, ea, S_, FH, S_, S_); }
  { EpiArgs ea{out, x1, nullptr, mod + 5 * D_, D_, 0};
    k_gemm<64, 64, 2, 2, EPI_OUT, 4><<<dim3(D_ / 64, S_ / 64, 1), 256, 4 * 16384, stream>>>(u, w_out, ea, FH, D_, FH, FH); }
}
```

```cpp
#include <hip/hip_runtime.h>
#include <hip/hip_bf16.h>
#include <stdint.h>

typedef unsigned short u16;
typedef __attribute__((ext_vector_type(8))) short bf16x8;
typedef __attribute__((ext_vector_type(4))) short s16x4;
typedef __attribute__((ext_vector_type(16))) float f32x16;
typedef __attribute__((ext_vector_type(4))) unsigned u32x4;

constexpr int S_ = 2048, D_ = 512, H_ = 32, DH = 128, HD = H_ * DH;
constexpr int F_ = 1792, FH = 896;

__device__ __forceinline__ float bf2f(u16 v) { return __uint_as_float((unsigned)v << 16); }
__device__ __forceinline__ u16 f2bf(float f) {
  unsigned u = __float_as_uint(f);
  u += 0x7fffu + ((u >> 16) & 1u);
  return (u16)(u >> 16);
}
__device__ __forceinline__ float rbf(float f) { return bf2f(f2bf(f)); }
__device__ __forceinline__ unsigned cvtpk(float lo, float hi) {
  unsigned r; asm volatile("v_cvt_pk_bf16_f32 %0, %1, %2" : "=v"(r) : "v"(lo), "v"(hi)); return r;
}
__device__ __forceinline__ int crow(int r, int hi) { return (r & 3) + 8 * (r >> 2) + 4 * hi; }
typedef __attribute__((ext_vector_type(4))) __bf16 bfv4;
typedef __attribute__((address_space(3))) bfv4 lds_bfv4;
__device__ __forceinline__ s16x4 tr_read_b(const void* lds_ptr) {
  bfv4 v = __builtin_amdgcn_ds_read_tr16_b64_v4bf16((lds_bfv4*)(lds_ptr));
  return *reinterpret_cast<s16x4*>(&v);
}
__device__ __forceinline__ float wave_sum(float v) {
#pragma unroll
  for (int o = 32; o >= 1; o >>= 1) v += __shfl_xor(v, o, 64);
  return v;
}

__global__ __launch_bounds__(256) void k_mod(const u16* __restrict__ c, const u16* __restrict__ w_mod, float* __restrict__ mod) {
  __shared__ float red[32][65];
  const int cg = threadIdx.x & 7, kg = threadIdx.x >> 3;
  float acc[8];
#pragma unroll
  for (int j = 0; j < 8; ++j) acc[j] = 0.f;
  bf16x8 wv[16];
#pragma unroll
  for (int k = 0; k < 16; ++k) wv[k] = *reinterpret_cast<const bf16x8*>(w_mod + (long)(kg * 16 + k) * 3072 + blockIdx.x * 64 + cg * 8);
#pragma unroll
  for (int k = 0; k < 16; ++k) {
    float cv = bf2f(c[kg * 16 + k]);
    float sv = rbf(cv / (1.f + __expf(-cv)));
#pragma unroll
    for (int j = 0; j < 8; ++j) acc[j] += sv * bf2f((u16)wv[k][j]);
  }
#pragma unroll
  for (int j = 0; j < 8; ++j) red[kg][cg * 8 + j] = acc[j];
  __syncthreads();
  if (threadIdx.x < 64) {
    float t = 0.f;
#pragma unroll
    for (int g = 0; g < 32; ++g) t += red[g][threadIdx.x];
    mod[blockIdx.x * 64 + threadIdx.x] = rbf(t);
  }
}

__global__ __launch_bounds__(256) void k_ln1(const u16* __restrict__ x, const float* __restrict__ mod, u16* __restrict__ h) {
  const int lane = threadIdx.x & 63, row = blockIdx.x * 4 + (threadIdx.x >> 6);
  bf16x8 xv = *reinterpret_cast<const bf16x8*>(x + (long)row * D_ + lane * 8);
  float v[8]; float s = 0.f;
#pragma unroll
  for (int j = 0; j < 8; ++j) { v[j] = bf2f((u16)xv[j]); s += v[j]; }
  float mean = wave_sum(s) * (1.f / D_);
  float q = 0.f;
#pragma unroll
  for (int j = 0; j < 8; ++j) { float d = v[j] - mean; q += d * d; }
  float rstd = rsqrtf(wave_sum(q) * (1.f / D_) + 1e-5f);
  bf16x8 o;
#pragma unroll
  for (int j = 0; j < 8; ++j) {
    int cidx = lane * 8 + j;
    o[j] = (short)f2bf((v[j] - mean) * rstd * (1.f + mod[D_ + cidx]) + mod[cidx]);
  }
  *reinterpret_cast<bf16x8*>(h + (long)row * D_ + lane * 8) = o;
}

enum { EPI_BF16 = 0, EPI_F32 = 1, EPI_GELU = 2, EPI_SP = 3, EPI_OUT = 4 };
struct EpiArgs {
  void* C;
  const u16* aux0;
  const u16* aux1;
  const float* auxf;
  int ldc;
  int pad;
};

__device__ __forceinline__ float gelu_tanh(float x) {
  const float k0 = 0.7978845608028654f, k1 = 0.044715f;
  float u = k0 * (x + k1 * x * x * x);
  float e = __expf(2.f * u);
  float t = 1.f - 2.f / (e + 1.f);
  return 0.5f * x * (1.f + t);
}

template <int N> __device__ __forceinline__ void wait_vm() { asm volatile("s_waitcnt vmcnt(%0)" :: "n"(N) : "memory"); }
__device__ __forceinline__ void glds16(const void* gsrc, unsigned lds_dst) {
  unsigned keep;
  asm volatile("s_mov_b32 %0, m0\n\ts_mov_b32 m0, %2\n\ts_nop 0\n\tglobal_load_lds_dwordx4 %1, off\n\ts_mov_b32 m0, %0"
               : "=&s"(keep) : "v"(gsrc), "s"(lds_dst) : "memory");
}
template <int BM, int BN, int WM, int WN, int EPI, int NST>
__global__ __launch_bounds__(WM * WN * 64) void k_gemm(const u16* __restrict__ A, const u16* __restrict__ B, EpiArgs ea,
                                                      int lda, int ldb, int K, int ksplit) {
  constexpr int BK = 64, NT = WM * WN * 64;
  constexpr int TM = BM / WM / 32, TN = BN / WN / 32;
  constexpr int ACH = BM * 8 / NT, BCH = BN * 8 / NT, G = ACH + BCH;
  constexpr int ABYTES = BM * BK * 2, BBYTES = BK * BN * 2, STB = ABYTES + BBYTES;
  extern __shared__ __attribute__((aligned(16))) char smem[];
  const int tid = threadIdx.x, lane = tid & 63, wid = tid >> 6;
  const int wm = wid / WN, wn = wid % WN;
  const int r32 = lane & 31, hi = lane >> 5;
  const int bm = blockIdx.y * BM, bn = blockIdx.x * BN;
  const int kbeg = blockIdx.z * ksplit;
  const int nt = ksplit / BK;
  A += (long)bm * lda + kbeg;
  B += (long)kbeg * ldb + bn;
  const unsigned lds0 = (unsigned)(uintptr_t)smem;
  const unsigned wid_u = __builtin_amdgcn_readfirstlane(wid);
  auto stage = [&](int t, int buf) {
    const unsigned sa = lds0 + buf * STB + wid_u * 1024;
    const unsigned sb = sa + ABYTES;
#pragma unroll
    for (int i = 0; i < ACH; ++i) {
      int c = tid + i * NT; int row = c >> 3, ch = (c & 7) ^ ((row >> 1) & 7);
      glds16(A + (long)row * lda + t * BK + ch * 8, sa + i * NT * 16);
    }
#pragma unroll
    for (int i = 0; i < BCH; ++i) {
      int c = tid + i * NT; int panel = c >> 8, row = (c & 255) >> 2, ch = c & 3;
      glds16(B + (long)(t * BK + row) * ldb + panel * 32 + ch * 8, sb + i * NT * 16);
    }
  };
  f32x16 acc[TM][TN];
#pragma unroll
  for (int m = 0; m < TM; ++m)
#pragma unroll
    for (int n = 0; n < TN; ++n) acc[m][n] = f32x16{};
#pragma unroll
  for (int s0 = 0; s0 < NST - 1; ++s0) stage(s0, s0);
  const int q4 = (lane & 15) >> 2, p4 = lane & 3, blk = (lane >> 4) & 1;
  int buf = 0, nbuf = NST - 1;
  for (int t = 0; t < nt; ++t) {
    if (t + NST - 2 < nt) wait_vm<(NST - 2) * G>(); else wait_vm<0>();
    __builtin_amdgcn_s_barrier();
    if (t + NST - 1 < nt) stage(t + NST - 1, nbuf);
    const char* sa = smem + buf * STB;
    const char* sb = sa + ABYTES;
#pragma unroll
    for (int s = 0; s < 4; ++s) {
      bf16x8 af[TM], bfr[TN];
#pragma unroll
      for (int m = 0; m < TM; ++m) {
        int row = (wm * TM + m) * 32 + r32; int ch = 2 * s + hi;
        af[m] = *reinterpret_cast<const bf16x8*>(sa + row * 128 + ((ch ^ ((row >> 1) & 7)) << 4));
      }
#pragma unroll
      for (int n = 0; n < TN; ++n) {
        const char* pb = sb + (wn * TN + n) * 4096 + (16 * s + 8 * hi + q4) * 64 + blk * 32 + p4 * 8;
        s16x4 lo = tr_read_b(pb), hh = tr_read_b(pb + 256);
        bfr[n] = (bf16x8){lo[0], lo[1], lo[2], lo[3], hh[0], hh[1], hh[2], hh[3]};
      }
#pragma unroll
      for (int m = 0; m < TM; ++m)
#pragma unroll
        for (int n = 0; n < TN; ++n)
          acc[m][n] = __builtin_amdgcn_mfma_f32_32x32x16_bf16(af[m], bfr[n], acc[m][n], 0, 0, 0);
    }
    asm volatile("s_waitcnt lgkmcnt(0)" ::: "memory");
    buf = (buf + 1 == NST) ? 0 : buf + 1;
    nbuf = (nbuf + 1 == NST) ? 0 : nbuf + 1;
  }
  constexpr int CLD = BN + 4;
  float* cs = reinterpret_cast<float*>(smem);
  __builtin_amdgcn_s_barrier();
#pragma unroll
  for (int m = 0; m < TM; ++m)
#pragma unroll
    for (int n = 0; n < TN; ++n)
#pragma unroll
      for (int r = 0; r < 16; ++r)
        cs[((wm * TM + m) * 32 + crow(r, hi)) * CLD + (wn * TN + n) * 32 + r32] = acc[m][n][r];
  __syncthreads();
  constexpr int NCH = BM * BN / 8 / NT;
  bf16x8 a0[NCH]; float bsp[NCH];
#pragma unroll
  for (int i = 0; i < NCH; ++i) {
    const int c = tid + i * NT, row = bm + c / (BN / 8), col = bn + (c % (BN / 8)) * 8;
    if constexpr (EPI == EPI_SP) { a0[i] = *reinterpret_cast<const bf16x8*>(ea.aux0 + (long)row * F_ + col); bsp[i] = bf2f(ea.aux1[row]); }
    if constexpr (EPI == EPI_OUT) a0[i] = *reinterpret_cast<const bf16x8*>(ea.aux0 + (long)row * D_ + col);
  }
#pragma unroll
  for (int i = 0; i < NCH; ++i) {
    const int c = tid + i * NT, lr = c / (BN / 8), lc = (c % (BN / 8)) * 8, row = bm + lr, col = bn + lc;
    const float4 u0 = *reinterpret_cast<const float4*>(cs + lr * CLD + lc), u1 = *reinterpret_cast<const float4*>(cs + lr * CLD + lc + 4);
    float v[8] = {u0.x, u0.y, u0.z, u0.w, u1.x, u1.y, u1.z, u1.w};
    if constexpr (EPI == EPI_F32) {
      float* dst = (float*)ea.C + ((long)blockIdx.z * S_ + row) * ea.ldc + col;
      *reinterpret_cast<float4*>(dst) = u0; *reinterpret_cast<float4*>(dst + 4) = u1;
    } else {
      bf16x8 o;
#pragma unroll
      for (int j = 0; j < 8; ++j) {
        float y;
        if constexpr (EPI == EPI_BF16) y = v[j];
        else if constexpr (EPI == EPI_GELU) y = gelu_tanh(rbf(v[j]));
        else if constexpr (EPI == EPI_SP) y = bf2f((u16)a0[i][j]) * rbf(rbf(v[j]) + bsp[i]);
        else y = bf2f((u16)a0[i][j]) + rbf(ea.auxf[col + j] * rbf(v[j]));
        o[j] = (short)f2bf(y);
      }
      *reinterpret_cast<bf16x8*>((u16*)ea.C + (long)row * ea.ldc + col) = o;
    }
  }
}

template <int NSPLIT>
__global__ __launch_bounds__(256) void k_resid_ln2(const u16* __restrict__ x, const float* __restrict__ part, const float* __restrict__ mod,
                                                   u16* __restrict__ x1o, u16* __restrict__ h2) {
  const int lane = threadIdx.x & 63, row = blockIdx.x * 4 + (threadIdx.x >> 6);
  bf16x8 xv = *reinterpret_cast<const bf16x8*>(x + (long)row * D_ + lane * 8);
  float a[8];
#pragma unroll
  for (int j = 0; j < 8; ++j) a[j] = 0.f;
#pragma unroll
  for (int p = 0; p < NSPLIT; ++p) {
    const float4* pp = reinterpret_cast<const float4*>(part + ((long)p * S_ + row) * D_ + lane * 8);
    float4 u0 = pp[0], u1 = pp[1];
    a[0] += u0.x; a[1] += u0.y; a[2] += u0.z; a[3] += u0.w; a[4] += u1.x; a[5] += u1.y; a[6] += u1.z; a[7] += u1.w;
  }
  float v[8]; float s = 0.f; bf16x8 xo;
#pragma unroll
  for (int j = 0; j < 8; ++j) {
    int cidx = lane * 8 + j;
    float t = rbf(bf2f((u16)xv[j]) + rbf(mod[2 * D_ + cidx] * rbf(a[j])));
    v[j] = t; s += t; xo[j] = (short)f2bf(t);
  }
  *reinterpret_cast<bf16x8*>(x1o + (long)row * D_ + lane * 8) = xo;
  float mean = wave_sum(s) * (1.f / D_);
  float q = 0.f;
#pragma unroll
  for (int j = 0; j < 8; ++j) { float d = v[j] - mean; q += d * d; }
  float rstd = rsqrtf(wave_sum(q) * (1.f / D_) + 1e-5f);
  bf16x8 o;
#pragma unroll
  for (int j = 0; j < 8; ++j) {
    int cidx = lane * 8 + j;
    o[j] = (short)f2bf((v[j] - mean) * rstd * (1.f + mod[4 * D_ + cidx]) + mod[3 * D_ + cidx]);
  }
  *reinterpret_cast<bf16x8*>(h2 + (long)row * D_ + lane * 8) = o;
}

__global__ __launch_bounds__(256) void k_ln_z2(const u16* __restrict__ z, u16* __restrict__ z2n) {
  const int lane = threadIdx.x & 63, row = blockIdx.x * 4 + (threadIdx.x >> 6);
  float v[16]; float s = 0.f; const bool act = lane < 56;
  bf16x8 c0 = {}, c1 = {};
  if (act) {
    c0 = *reinterpret_cast<const bf16x8*>(z + (long)row * F_ + FH + lane * 16);
    c1 = *reinterpret_cast<const bf16x8*>(z + (long)row * F_ + FH + lane * 16 + 8);
  }
#pragma unroll
  for (int j = 0; j < 8; ++j) { v[j] = bf2f((u16)c0[j]); v[8 + j] = bf2f((u16)c1[j]); s += v[j] + v[8 + j]; }
  float mean = wave_sum(s) * (1.f / FH);
  float q = 0.f;
  if (act) {
#pragma unroll
    for (int j = 0; j < 16; ++j) { float d = v[j] - mean; q += d * d; }
  }
  float rstd = rsqrtf(wave_sum(q) * (1.f / FH) + 1e-5f);
  if (act) {
    bf16x8 o0, o1;
#pragma unroll
    for (int j = 0; j < 8; ++j) { o0[j] = (short)f2bf((v[j] - mean) * rstd); o1[j] = (short)f2bf((v[8 + j] - mean) * rstd); }
    *reinterpret_cast<bf16x8*>(z2n + (long)row * FH + lane * 16) = o0;
    *reinterpret_cast<bf16x8*>(z2n + (long)row * FH + lane * 16 + 8) = o1;
  }
}

constexpr int NW = 8, QBLK = 32, KVBLK = 64;
constexpr float SCALE = 0.088388347648318440f;
constexpr float THR = 8.f;
constexpr size_t SHM_V = KVBLK * DH * 2, SHM_K = KVBLK * DH * 2, SHM_ATTN = 2 * SHM_V + 2 * SHM_K + NW * 64 * 4;
#define KSWZ(row, colB) ((row) * 256 + ((colB) ^ (((row) & 7) << 4)))
#define SBAR() __builtin_amdgcn_sched_barrier(0)

__device__ __forceinline__ void partialSM(f32x16& p0, f32x16& p1, float& m_reg, float& mn, float& alpha) {
  constexpr float C = SCALE * 1.4426950408889634f;
  float pmax = p0[0];
  for (int r = 1; r < 16; ++r) pmax = fmaxf(pmax, p0[r]);
  for (int r = 0; r < 16; ++r) pmax = fmaxf(pmax, p1[r]);
  { auto rr = __builtin_amdgcn_permlane32_swap(__float_as_uint(pmax), __float_as_uint(pmax), false, false);
    pmax = fmaxf(__uint_as_float(rr[0]), __uint_as_float(rr[1])); }
  if (__builtin_expect(__all(pmax - m_reg <= THR / SCALE), 1)) { mn = m_reg; alpha = 1.f; }
  else { mn = fmaxf(m_reg, pmax); alpha = __builtin_amdgcn_exp2f((m_reg - mn) * C); m_reg = mn; }
  float mnC = -mn * C;
  for (int r = 0; r < 16; ++r) p0[r] = fmaf(p0[r], C, mnC);
  for (int r = 0; r < 16; ++r) p1[r] = fmaf(p1[r], C, mnC);
  for (int r = 0; r < 16; ++r) p0[r] = __builtin_amdgcn_exp2f(p0[r]);
}
__device__ __forceinline__ void finishSM(f32x16& p0, f32x16& p1, float alpha, float& l_reg, bf16x8& pa0, bf16x8& pa1, bf16x8& pa2, bf16x8& pa3) {
  for (int r = 0; r < 16; ++r) p1[r] = __builtin_amdgcn_exp2f(p1[r]);
  float ps = 0;
  for (int r = 0; r < 16; ++r) ps += p0[r];
  for (int r = 0; r < 16; ++r) ps += p1[r];
  { auto rr = __builtin_amdgcn_permlane32_swap(__float_as_uint(ps), __float_as_uint(ps), false, false);
    ps = __uint_as_float(rr[0]) + __uint_as_float(rr[1]); }
  l_reg = l_reg * alpha + ps;
#define PK4(P, BASE, OUT) do { unsigned a0 = cvtpk(P[BASE + 0], P[BASE + 1]), a1 = cvtpk(P[BASE + 2], P[BASE + 3]);   \
    unsigned b0 = cvtpk(P[BASE + 4], P[BASE + 5]), b1 = cvtpk(P[BASE + 6], P[BASE + 7]);                              \
    auto r0 = __builtin_amdgcn_permlane32_swap(a0, b0, false, false); auto r1 = __builtin_amdgcn_permlane32_swap(a1, b1, false, false); \
    u32x4 w = {r0[0], r1[0], r0[1], r1[1]}; OUT = *reinterpret_cast<bf16x8*>(&w); } while (0)
  PK4(p0, 0, pa0); PK4(p0, 8, pa1); PK4(p1, 0, pa2); PK4(p1, 8, pa3);
#undef PK4
}
__device__ __forceinline__ void qkt(f32x16& p0, f32x16& p1, const u16* Ks, const bf16x8* qr, int r32, int hi) {
  p0 = f32x16{}; p1 = f32x16{};
  for (int d0 = 0; d0 < 8; ++d0) { int cb = (d0 * 16 + hi * 8) * 2;
    bf16x8 b0 = *reinterpret_cast<const bf16x8*>((const char*)Ks + KSWZ(r32, cb));
    bf16x8 b1 = *reinterpret_cast<const bf16x8*>((const char*)Ks + KSWZ(32 + r32, cb));
    p0 = __builtin_amdgcn_mfma_f32_32x32x16_bf16(b0, qr[d0], p0, 0, 0, 0);
    p1 = __builtin_amdgcn_mfma_f32_32x32x16_bf16(b1, qr[d0], p1, 0, 0, 0); }
}
__device__ __forceinline__ int v_st(int k, int c) { const int kk = (k & ~0xC) | ((k & 4) << 1) | ((k & 8) >> 1); return ((kk >> 3) * 4 + (c >> 5)) * 512 + ((kk & 7) * 32 + (c & 31)) * 2; }
__device__ __forceinline__ int v_rd_base(int lane) { return ((lane & 3) << 3) | (((lane >> 2) & 3) << 6) | (((lane >> 4) & 1) << 5) | (((lane >> 5) & 1) << 8); }
constexpr int v_rd_off(int d0, int ks, int half) { return d0 * 512 + ks * 4096 + half * 2048; }
template <int OFF> __device__ __forceinline__ s16x4 tr_read(int vb) {
  s16x4 r; asm volatile("ds_read_b64_tr_b16 %0, %1 offset:%2" : "=&v"(r) : "v"(vb), "i"(OFF) : "memory"); return r;
}
template <int D0> __device__ __forceinline__ void pv_one(f32x16& od, int vb, bf16x8 pa0, bf16x8 pa1, bf16x8 pa2, bf16x8 pa3) {
  const s16x4 l0 = tr_read<v_rd_off(D0, 0, 0)>(vb), h0 = tr_read<v_rd_off(D0, 0, 1)>(vb), l1 = tr_read<v_rd_off(D0, 1, 0)>(vb), h1 = tr_read<v_rd_off(D0, 1, 1)>(vb);
  const s16x4 l2 = tr_read<v_rd_off(D0, 2, 0)>(vb), h2 = tr_read<v_rd_off(D0, 2, 1)>(vb), l3 = tr_read<v_rd_off(D0, 3, 0)>(vb), h3 = tr_read<v_rd_off(D0, 3, 1)>(vb);
  asm volatile("s_waitcnt lgkmcnt(0)" ::: "memory"); SBAR();
#define PK(L, H) (bf16x8){L[0], L[1], L[2], L[3], H[0], H[1], H[2], H[3]}
  od = __builtin_amdgcn_mfma_f32_32x32x16_bf16(pa0, PK(l0, h0), od, 0, 0, 0);
  od = __builtin_amdgcn_mfma_f32_32x32x16_bf16(pa1, PK(l1, h1), od, 0, 0, 0);
  od = __builtin_amdgcn_mfma_f32_32x32x16_bf16(pa2, PK(l2, h2), od, 0, 0, 0);
  od = __builtin_amdgcn_mfma_f32_32x32x16_bf16(pa3, PK(l3, h3), od, 0, 0, 0);
#undef PK
}
__device__ __forceinline__ void pv_d0(f32x16* o, int vb, bf16x8 pa0, bf16x8 pa1, bf16x8 pa2, bf16x8 pa3) {
  pv_one<0>(o[0], vb, pa0, pa1, pa2, pa3); pv_one<1>(o[1], vb, pa0, pa1, pa2, pa3); pv_one<2>(o[2], vb, pa0, pa1, pa2, pa3); pv_one<3>(o[3], vb, pa0, pa1, pa2, pa3);
}

constexpr size_t SHM_FUSED = 131072;
__global__ __launch_bounds__(512, 1) void k_attn(const u16* __restrict__ X, const u16* __restrict__ Wq, const float* __restrict__ mod,
                                                 const u16* __restrict__ Kc, const u16* __restrict__ Vc, u16* __restrict__ O) {
  extern __shared__ __attribute__((aligned(16))) char lds[];
  const int b = blockIdx.x;
  const int head = (b & 7) + 8 * ((b >> 3) & 3), qb = b >> 5;
  const u16* Kh = Kc + (long)head * S_ * DH;
  const u16* Vh = Vc + (long)head * S_ * DH;
  u16* Ob = O + (long)qb * 256 * HD + head * DH;
  const int tid = threadIdx.x, wid = tid >> 6, lane = tid & 63, r32 = lane & 31, hi = lane >> 5;
  u16* V_lds = (u16*)lds; u16* K_lds = (u16*)(lds + 2 * SHM_V);
  float* ws = (float*)(lds + 2 * SHM_V + 2 * SHM_K) + wid * 64; float* li_l = ws; float* al_l = ws + 32;
  float m_reg = -1e30f, l_reg = 0; f32x16 o[4] = {}; bf16x8 qr[8];
  {
    const unsigned l0 = (unsigned)(uintptr_t)lds + __builtin_amdgcn_readfirstlane(wid) * 1024;
#pragma unroll
    for (int i = 0; i < 16; ++i) {
      const int c = tid + i * 512, panel = c >> 11, row = (c & 2047) >> 2, ch = c & 3;
      glds16(Wq + (long)row * HD + head * DH + panel * 32 + ch * 8, l0 + i * 8192);
    }
    const u16* xr = X + (long)(qb * 256 + wid * QBLK + r32) * D_ + hi * 64;
    bf16x8 xf[32];
#pragma unroll
    for (int ks = 0; ks < 32; ++ks) xf[ks] = *reinterpret_cast<const bf16x8*>(xr + (ks >> 3) * 128 + (ks & 7) * 8);
    asm volatile("s_waitcnt vmcnt(0)" ::: "memory");
    __syncthreads();
    f32x16 qa[4] = {};
    const char* wb = lds + (64 * hi + ((lane & 15) >> 2)) * 64 + ((lane >> 4) & 1) * 32 + (lane & 3) * 8;
#pragma unroll
    for (int ks = 0; ks < 32; ++ks) {
      const int k0 = (ks >> 3) * 128 + (ks & 7) * 8;
#pragma unroll
      for (int bl = 0; bl < 4; ++bl) {
        const char* pb = wb + bl * 32768 + k0 * 64;
        s16x4 lo = tr_read_b(pb), hh = tr_read_b(pb + 256);
        bf16x8 af = (bf16x8){lo[0], lo[1], lo[2], lo[3], hh[0], hh[1], hh[2], hh[3]};
        qa[bl] = __builtin_amdgcn_mfma_f32_32x32x16_bf16(af, xf[ks], qa[bl], 0, 0, 0);
      }
    }
    SBAR(); asm volatile("s_nop 15\n\ts_nop 15" ::: "memory"); SBAR();
#define PKQ(P, BASE, OUT) do { unsigned a0 = cvtpk(P[BASE + 0], P[BASE + 1]), a1 = cvtpk(P[BASE + 2], P[BASE + 3]);   \
    unsigned b0 = cvtpk(P[BASE + 4], P[BASE + 5]), b1 = cvtpk(P[BASE + 6], P[BASE + 7]);                              \
    auto r0 = __builtin_amdgcn_permlane32_swap(a0, b0, false, false); auto r1 = __builtin_amdgcn_permlane32_swap(a1, b1, false, false); \
    u32x4 w = {r0[0], r1[0], r0[1], r1[1]}; OUT = *reinterpret_cast<bf16x8*>(&w); } while (0)
#pragma unroll
    for (int bl = 0; bl < 4; ++bl) { PKQ(qa[bl], 0, qr[2 * bl]); PKQ(qa[bl], 8, qr[2 * bl + 1]); }
#undef PKQ
    __syncthreads();
  }
  const int sr = tid >> 4, sc = (tid & 15) * 8, vst0 = v_st(sr, sc), vst1 = v_st(32 + sr, sc);
  const int vb0 = (int)(uintptr_t)V_lds + v_rd_base(lane);
  struct { bf16x8 vs0, vs1, ks0, ks1; } sr_[2];
#define LD8(p) (*reinterpret_cast<const bf16x8*>(p))
#define SLOAD(i, k0) do { sr_[i].vs0 = LD8(&Vh[(long)((k0) + sr) * DH + sc]); sr_[i].vs1 = LD8(&Vh[(long)((k0) + 32 + sr) * DH + sc]); \
    sr_[i].ks0 = LD8(&Kh[(long)((k0) + sr) * DH + sc]); sr_[i].ks1 = LD8(&Kh[(long)((k0) + 32 + sr) * DH + sc]); } while (0)
#define SWRITE(b, i) do { *(bf16x8*)((char*)V_lds + (b) * SHM_V + vst0) = sr_[i].vs0;          \
    *(bf16x8*)((char*)V_lds + (b) * SHM_V + vst1) = sr_[i].vs1; int kc = sc * 2;               \
    *(bf16x8*)((char*)K_lds + (b) * SHM_K + KSWZ(sr, kc)) = sr_[i].ks0;                       \
    *(bf16x8*)((char*)K_lds + (b) * SHM_K + KSWZ(32 + sr, kc)) = sr_[i].ks1; } while (0)
#define SWAIT() asm volatile("s_waitcnt vmcnt(4)" ::: "memory")
#define RESC(a) do { if (__any((a) < 1.f)) { if (hi == 0) al_l[r32] = (a); asm volatile("s_waitcnt lgkmcnt(0)" ::: "memory"); \
    for (int d = 0; d < 4; ++d) for (int r = 0; r < 16; ++r) o[d][r] *= al_l[crow(r, hi)]; } } while (0)
  f32x16 pA0, pA1, pB0, pB1; float mnA, mnB, alA, alB; bf16x8 pa0, pa1, pa2, pa3; constexpr int NT = S_ / KVBLK;
  constexpr int SE = 0, SO = 1;
  SLOAD(SE, 0); asm volatile("s_waitcnt vmcnt(0)" ::: "memory"); SWRITE(0, SE); __syncthreads();
  qkt(pA0, pA1, K_lds, qr, r32, hi); partialSM(pA0, pA1, m_reg, mnA, alA);
  SLOAD(SO, KVBLK); SLOAD(SE, 2 * KVBLK);
  SWAIT(); SWRITE(1, SO); __syncthreads();
  for (int j = 1; j + 1 < NT; j += 2) {
    SBAR(); qkt(pB0, pB1, (u16*)((char*)K_lds + SHM_K), qr, r32, hi);
    finishSM(pA0, pA1, alA, l_reg, pa0, pa1, pa2, pa3); SBAR();
    SLOAD(SO, (j + 2) * KVBLK); SBAR();
    pv_d0(o, vb0, pa0, pa1, pa2, pa3); partialSM(pB0, pB1, m_reg, mnB, alB);
    __syncthreads(); SWAIT(); SWRITE(0, SE);
    RESC(alB); __syncthreads();
    SBAR(); qkt(pA0, pA1, K_lds, qr, r32, hi);
    finishSM(pB0, pB1, alB, l_reg, pa0, pa1, pa2, pa3); SBAR();
    if (j + 3 < NT) SLOAD(SE, (j + 3) * KVBLK); SBAR();
    pv_d0(o, vb0 + (int)SHM_V, pa0, pa1, pa2, pa3); partialSM(pA0, pA1, m_reg, mnA, alA);
    __syncthreads(); SWAIT(); SWRITE(1, SO);
    RESC(alA); __syncthreads();
  }
  SBAR(); qkt(pB0, pB1, (u16*)((char*)K_lds + SHM_K), qr, r32, hi);
  finishSM(pA0, pA1, alA, l_reg, pa0, pa1, pa2, pa3); SBAR();
  pv_d0(o, vb0, pa0, pa1, pa2, pa3); partialSM(pB0, pB1, m_reg, mnB, alB);
  __syncthreads(); RESC(alB);
  finishSM(pB0, pB1, alB, l_reg, pa0, pa1, pa2, pa3); SBAR();
  pv_d0(o, vb0 + (int)SHM_V, pa0, pa1, pa2, pa3);
  if (hi == 0) li_l[r32] = l_reg; asm volatile("s_waitcnt lgkmcnt(0)" ::: "memory");
  float rli[16];
#pragma unroll
  for (int r = 0; r < 16; ++r) rli[r] = __builtin_amdgcn_rcpf(li_l[crow(r, hi)]);
  u16* Ow = Ob + (long)(wid * QBLK) * HD;
#pragma unroll
  for (int r = 0; r < 16; ++r) { int orow = crow(r, hi);
#pragma unroll
    for (int d0 = 0; d0 < 4; ++d0) Ow[(long)orow * HD + d0 * 32 + r32] = f2bf(o[d0][r] * rli[r]); }
#undef SLOAD
#undef SWRITE
#undef SWAIT
#undef RESC
}

extern "C" void kernel_launch(void* const* d_in, const int* in_sizes, int n_in, void* d_out, int out_size, void* d_ws, size_t ws_size,
                              hipStream_t stream) {
  const u16* x = (const u16*)d_in[0];
  const u16* kc = (const u16*)d_in[1];
  const u16* vc = (const u16*)d_in[2];
  const u16* wq = (const u16*)d_in[3];
  const u16* wo = (const u16*)d_in[4];
  const u16* w_in = (const u16*)d_in[6];
  const u16* w_sp = (const u16*)d_in[7];
  const u16* b_sp = (const u16*)d_in[8];
  const u16* w_out = (const u16*)d_in[9];
  const u16* c = (const u16*)d_in[10];
  const u16* w_mod = (const u16*)d_in[11];
  u16* out = (u16*)d_out;

  char* ws = (char*)d_ws;
  size_t off = 0;
  auto carve = [&](size_t bytes) { void* p = ws + off; off += (bytes + 255) / 256 * 256; return p; };
  float* mod = (float*)carve(3072 * 4);
  u16* h1 = (u16*)carve((size_t)S_ * D_ * 2);
  u16* q = (u16*)carve((size_t)S_ * HD * 2);
  u16* o = (u16*)carve((size_t)S_ * HD * 2);
  float* part = (float*)carve((size_t)4 * S_ * D_ * 4);
  u16* x1 = (u16*)carve((size_t)S_ * D_ * 2);
  u16* h2 = (u16*)carve((size_t)S_ * D_ * 2);
  u16* z = (u16*)carve((size_t)S_ * F_ * 2);
  u16* z2n = (u16*)carve((size_t)S_ * FH * 2);
  u16* u = (u16*)carve((size_t)S_ * FH * 2);

  constexpr int GS = 2 * (128 * 64 * 2 + 64 * 128 * 2);
  hipFuncSetAttribute((const void*)k_gemm<64, 128, 2, 2, EPI_BF16, 3>, hipFuncAttributeMaxDynamicSharedMemorySize, 3 * 24576);
  hipFuncSetAttribute((const void*)k_gemm<64, 128, 2, 2, EPI_F32, 3>, hipFuncAttributeMaxDynamicSharedMemorySize, 3 * 24576);
  hipFuncSetAttribute((const void*)k_gemm<64, 128, 2, 2, EPI_GELU, 3>, hipFuncAttributeMaxDynamicSharedMemorySize, 3 * 24576);
  hipFuncSetAttribute((const void*)k_gemm<64, 128, 2, 2, EPI_SP, 4>, hipFuncAttributeMaxDynamicSharedMemorySize, 4 * 24576);
  hipFuncSetAttribute((const void*)k_gemm<64, 64, 2, 2, EPI_OUT, 4>, hipFuncAttributeMaxDynamicSharedMemorySize, 4 * 16384);
  hipFuncSetAttribute((const void*)k_attn, hipFuncAttributeMaxDynamicSharedMemorySize, (int)SHM_FUSED);

  k_mod<<<48, 256, 0, stream>>>(c, w_mod, mod);
  k_ln1<<<S_ / 4, 256, 0, stream>>>(x, mod, h1);
  k_attn<<<256, 512, SHM_FUSED, stream>>>(h1, wq, mod, kc, vc, o);
  { EpiArgs ea{part, nullptr, nullptr, nullptr, D_, 0};
    k_gemm<64, 128, 2, 2, EPI_F32, 3><<<dim3(D_ / 128, S_ / 64, 4), 256, 3 * 24576, stream>>>(o, wo, ea, HD, D_, HD, HD / 4); }
  k_resid_ln2<4><<<S_ / 4, 256, 0, stream>>>(x, part, mod, x1, h2);
  { EpiArgs ea{z, nullptr, nullptr, nullptr, F_, 0};
    k_gemm<64, 128, 2, 2, EPI_GELU, 3><<<dim3(F_ / 128, S_ / 64, 1), 256, 3 * 24576, stream>>>(h2, w_in, ea, D_, F_, D_, D_); }
  k_ln_z2<<<S_ / 4, 256, 0, stream>>>(z, z2n);
  { EpiArgs ea{u, z, b_sp, nullptr, FH, 0};
    k_gemm<64, 128, 2, 2, EPI_SP, 4><<<dim3(FH / 128, S_ / 64, 1), 256, 4 * 24576, stream>>>(w_sp, z2n, ea, S_, FH, S_, S_); }
  { EpiArgs ea{out, x1, nullptr, mod + 5 * D_, D_, 0};
    k_gemm<64, 64, 2, 2, EPI_OUT, 4><<<dim3(D_ / 64, S_ / 64, 1), 256, 4 * 16384, stream>>>(u, w_out, ea, FH, D_, FH, FH); }
}
```

```cpp
#include <hip/hip_runtime.h>
#include <hip/hip_bf16.h>
#include <stdint.h>

typedef unsigned short u16;
typedef __attribute__((ext_vector_type(8))) short bf16x8;
typedef __attribute__((ext_vector_type(4))) short s16x4;
typedef __attribute__((ext_vector_type(16))) float f32x16;
typedef __attribute__((ext_vector_type(4))) unsigned u32x4;

constexpr int S_ = 2048, D_ = 512, H_ = 32, DH = 128, HD = H_ * DH;
constexpr int F_ = 1792, FH = 896;

__device__ __forceinline__ float bf2f(u16 v) { return __uint_as_float((unsigned)v << 16); }
__device__ __forceinline__ u16 f2bf(float f) {
  unsigned u = __float_as_uint(f);
  u += 0x7fffu + ((u >> 16) & 1u);
  return (u16)(u >> 16);
}
__device__ __forceinline__ float rbf(float f) { return bf2f(f2bf(f)); }
__device__ __forceinline__ unsigned cvtpk(float lo, float hi) {
  unsigned r; asm volatile("v_cvt_pk_bf16_f32 %0, %1, %2" : "=v"(r) : "v"(lo), "v"(hi)); return r;
}
__device__ __forceinline__ int crow(int r, int hi) { return (r & 3) + 8 * (r >> 2) + 4 * hi; }
typedef __attribute__((ext_vector_type(4))) __bf16 bfv4;
typedef __attribute__((address_space(3))) bfv4 lds_bfv4;
__device__ __forceinline__ s16x4 tr_read_b(const void* lds_ptr) {
  bfv4 v = __builtin_amdgcn_ds_read_tr16_b64_v4bf16((lds_bfv4*)(lds_ptr));
  return *reinterpret_cast<s16x4*>(&v);
}
__device__ __forceinline__ float wave_sum(float v) {
#pragma unroll
  for (int o = 32; o >= 1; o >>= 1) v += __shfl_xor(v, o, 64);
  return v;
}

__global__ __launch_bounds__(256) void k_mod(const u16* __restrict__ c, const u16* __restrict__ w_mod, float* __restrict__ mod) {
  __shared__ float red[32][65];
  const int cg = threadIdx.x & 7, kg = threadIdx.x >> 3;
  float acc[8];
#pragma unroll
  for (int j = 0; j < 8; ++j) acc[j] = 0.f;
  bf16x8 wv[16];
#pragma unroll
  for (int k = 0; k < 16; ++k) wv[k] = *reinterpret_cast<const bf16x8*>(w_mod + (long)(kg * 16 + k) * 3072 + blockIdx.x * 64 + cg * 8);
#pragma unroll
  for (int k = 0; k < 16; ++k) {
    float cv = bf2f(c[kg * 16 + k]);
    float sv = rbf(cv / (1.f + __expf(-cv)));
#pragma unroll
    for (int j = 0; j < 8; ++j) acc[j] += sv * bf2f((u16)wv[k][j]);
  }
#pragma unroll
  for (int j = 0; j < 8; ++j) red[kg][cg * 8 + j] = acc[j];
  __syncthreads();
  if (threadIdx.x < 64) {
    float t = 0.f;
#pragma unroll
    for (int g = 0; g < 32; ++g) t += red[g][threadIdx.x];
    mod[blockIdx.x * 64 + threadIdx.x] = rbf(t);
  }
}

__global__ __launch_bounds__(256) void k_ln1(const u16* __restrict__ x, const float* __restrict__ mod, u16* __restrict__ h) {
  const int lane = threadIdx.x & 63, row = blockIdx.x * 4 + (threadIdx.x >> 6);
  bf16x8 xv = *reinterpret_cast<const bf16x8*>(x + (long)row * D_ + lane * 8);
  float v[8]; float s = 0.f;
#pragma unroll
  for (int j = 0; j < 8; ++j) { v[j] = bf2f((u16)xv[j]); s += v[j]; }
  float mean = wave_sum(s) * (1.f / D_);
  float q = 0.f;
#pragma unroll
  for (int j = 0; j < 8; ++j) { float d = v[j] - mean; q += d * d; }
  float rstd = rsqrtf(wave_sum(q) * (1.f / D_) + 1e-5f);
  bf16x8 o;
#pragma unroll
  for (int j = 0; j < 8; ++j) {
    int cidx = lane * 8 + j;
    o[j] = (short)f2bf((v[j] - mean) * rstd * (1.f + mod[D_ + cidx]) + mod[cidx]);
  }
  *reinterpret_cast<bf16x8*>(h + (long)row * D_ + lane * 8) = o;
}

enum { EPI_BF16 = 0, EPI_F32 = 1, EPI_GELU = 2, EPI_SP = 3, EPI_OUT = 4 };
struct EpiArgs {
  void* C;
  const u16* aux0;
  const u16* aux1;
  const float* auxf;
  int ldc;
  int pad;
};

__device__ __forceinline__ float gelu_tanh(float x) {
  const float k0 = 0.7978845608028654f, k1 = 0.044715f;
  float u = k0 * (x + k1 * x * x * x);
  float e = __expf(2.f * u);
  float t = 1.f - 2.f / (e + 1.f);
  return 0.5f * x * (1.f + t);
}

template <int N> __device__ __forceinline__ void wait_vm() { asm volatile("s_waitcnt vmcnt(%0)" :: "n"(N) : "memory"); }
__device__ __forceinline__ void glds16(const void* gsrc, unsigned lds_dst) {
  unsigned keep;
  asm volatile("s_mov_b32 %0, m0\n\ts_mov_b32 m0, %2\n\ts_nop 0\n\tglobal_load_lds_dwordx4 %1, off\n\ts_mov_b32 m0, %0"
               : "=&s"(keep) : "v"(gsrc), "s"(lds_dst) : "memory");
}
template <int BM, int BN, int WM, int WN, int EPI, int NST>
__global__ __launch_bounds__(WM * WN * 64) void k_gemm(const u16* __restrict__ A, const u16* __restrict__ B, EpiArgs ea,
                                                      int lda, int ldb, int K, int ksplit) {
  constexpr int BK = 64, NT = WM * WN * 64;
  constexpr int TM = BM / WM / 32, TN = BN / WN / 32;
  constexpr int ACH = BM * 8 / NT, BCH = BN * 8 / NT, G = ACH + BCH;
  constexpr int ABYTES = BM * BK * 2, BBYTES = BK * BN * 2, STB = ABYTES + BBYTES;
  extern __shared__ __attribute__((aligned(16))) char smem[];
  const int tid = threadIdx.x, lane = tid & 63, wid = tid >> 6;
  const int wm = wid / WN, wn = wid % WN;
  const int r32 = lane & 31, hi = lane >> 5;
  const int bm = blockIdx.y * BM, bn = blockIdx.x * BN;
  const int kbeg = blockIdx.z * ksplit;
  const int nt = ksplit / BK;
  A += (long)bm * lda + kbeg;
  B += (long)kbeg * ldb + bn;
  const unsigned lds0 = (unsigned)(uintptr_t)smem;
  const unsigned wid_u = __builtin_amdgcn_readfirstlane(wid);
  auto stage = [&](int t, int buf) {
    const unsigned sa = lds0 + buf * STB + wid_u * 1024;
    const unsigned sb = sa + ABYTES;
#pragma unroll
    for (int i = 0; i < ACH; ++i) {
      int c = tid + i * NT; int row = c >> 3, ch = (c & 7) ^ ((row >> 1) & 7);
      glds16(A + (long)row * lda + t * BK + ch * 8, sa + i * NT * 16);
    }
#pragma unroll
    for (int i = 0; i < BCH; ++i) {
      int c = tid + i * NT; int panel = c >> 8, row = (c & 255) >> 2, ch = c & 3;
      glds16(B + (long)(t * BK + row) * ldb + panel * 32 + ch * 8, sb + i * NT * 16);
    }
  };
  f32x16 acc[TM][TN];
#pragma unroll
  for (int m = 0; m < TM; ++m)
#pragma unroll
    for (int n = 0; n < TN; ++n) acc[m][n] = f32x16{};
#pragma unroll
  for (int s0 = 0; s0 < NST - 1; ++s0) stage(s0, s0);
  const int q4 = (lane & 15) >> 2, p4 = lane & 3, blk = (lane >> 4) & 1;
  int buf = 0, nbuf = NST - 1;
  for (int t = 0; t < nt; ++t) {
    if (t + NST - 2 < nt) wait_vm<(NST - 2) * G>(); else wait_vm<0>();
    __builtin_amdgcn_s_barrier();
    if (t + NST - 1 < nt) stage(t + NST - 1, nbuf);
    const char* sa = smem + buf * STB;
    const char* sb = sa + ABYTES;
#pragma unroll
    for (int s = 0; s < 4; ++s) {
      bf16x8 af[TM], bfr[TN];
#pragma unroll
      for (int m = 0; m < TM; ++m) {
        int row = (wm * TM + m) * 32 + r32; int ch = 2 * s + hi;
        af[m] = *reinterpret_cast<const bf16x8*>(sa + row * 128 + ((ch ^ ((row >> 1) & 7)) << 4));
      }
#pragma unroll
      for (int n = 0; n < TN; ++n) {
        const char* pb = sb + (wn * TN + n) * 4096 + (16 * s + 8 * hi + q4) * 64 + blk * 32 + p4 * 8;
        s16x4 lo = tr_read_b(pb), hh = tr_read_b(pb + 256);
        bfr[n] = (bf16x8){lo[0], lo[1], lo[2], lo[3], hh[0], hh[1], hh[2], hh[3]};
      }
#pragma unroll
      for (int m = 0; m < TM; ++m)
#pragma unroll
        for (int n = 0; n < TN; ++n)
          acc[m][n] = __builtin_amdgcn_mfma_f32_32x32x16_bf16(af[m], bfr[n], acc[m][n], 0, 0, 0);
    }
    asm volatile("s_waitcnt lgkmcnt(0)" ::: "memory");
    buf = (buf + 1 == NST) ? 0 : buf + 1;
    nbuf = (nbuf + 1 == NST) ? 0 : nbuf + 1;
  }
  constexpr int CLD = BN + 4;
  float* cs = reinterpret_cast<float*>(smem);
  __builtin_amdgcn_s_barrier();
#pragma unroll
  for (int m = 0; m < TM; ++m)
#pragma unroll
    for (int n = 0; n < TN; ++n)
#pragma unroll
      for (int r = 0; r < 16; ++r)
        cs[((wm * TM + m) * 32 + crow(r, hi)) * CLD + (wn * TN + n) * 32 + r32] = acc[m][n][r];
  __syncthreads();
  constexpr int NCH = BM * BN / 8 / NT;
  bf16x8 a0[NCH]; float bsp[NCH];
#pragma unroll
  for (int i = 0; i < NCH; ++i) {
    const int c = tid + i * NT, row = bm + c / (BN / 8), col = bn + (c % (BN / 8)) * 8;
    if constexpr (EPI == EPI_SP) { a0[i] = *reinterpret_cast<const bf16x8*>(ea.aux0 + (long)row * F_ + col); bsp[i] = bf2f(ea.aux1[row]); }
    if constexpr (EPI == EPI_OUT) a0[i] = *reinterpret_cast<const bf16x8*>(ea.aux0 + (long)row * D_ + col);
  }
#pragma unroll
  for (int i = 0; i < NCH; ++i) {
    const int c = tid + i * NT, lr = c / (BN / 8), lc = (c % (BN / 8)) * 8, row = bm + lr, col = bn + lc;
    const float4 u0 = *reinterpret_cast<const float4*>(cs + lr * CLD + lc), u1 = *reinterpret_cast<const float4*>(cs + lr * CLD + lc + 4);
    float v[8] = {u0.x, u0.y, u0.z, u0.w, u1.x, u1.y, u1.z, u1.w};
    if constexpr (EPI == EPI_F32) {
      float* dst = (float*)ea.C + ((long)blockIdx.z * S_ + row) * ea.ldc + col;
      *reinterpret_cast<float4*>(dst) = u0; *reinterpret_cast<float4*>(dst + 4) = u1;
    } else {
      bf16x8 o;
#pragma unroll
      for (int j = 0; j < 8; ++j) {
        float y;
        if constexpr (EPI == EPI_BF16) y = v[j];
        else if constexpr (EPI == EPI_GELU) y = gelu_tanh(rbf(v[j]));
        else if constexpr (EPI == EPI_SP) y = bf2f((u16)a0[i][j]) * rbf(rbf(v[j]) + bsp[i]);
        else y = bf2f((u16)a0[i][j]) + rbf(ea.auxf[col + j] * rbf(v[j]));
        o[j] = (short)f2bf(y);
      }
      *reinterpret_cast<bf16x8*>((u16*)ea.C + (long)row * ea.ldc + col) = o;
    }
  }
}

template <int NSPLIT>
__global__ __launch_bounds__(256) void k_resid_ln2(const u16* __restrict__ x, const float* __restrict__ part, const float* __restrict__ mod,
                                                   u16* __restrict__ x1o, u16* __restrict__ h2) {
  const int lane = threadIdx.x & 63, row = blockIdx.x * 4 + (threadIdx.x >> 6);
  bf16x8 xv = *reinterpret_cast<const bf16x8*>(x + (long)row * D_ + lane * 8);
  float a[8];
#pragma unroll
  for (int j = 0; j < 8; ++j) a[j] = 0.f;
#pragma unroll
  for (int p = 0; p < NSPLIT; ++p) {
    const float4* pp = reinterpret_cast<const float4*>(part + ((long)p * S_ + row) * D_ + lane * 8);
    float4 u0 = pp[0], u1 = pp[1];
    a[0] += u0.x; a[1] += u0.y; a[2] += u0.z; a[3] += u0.w; a[4] += u1.x; a[5] += u1.y; a[6] += u1.z; a[7] += u1.w;
  }
  float v[8]; float s = 0.f; bf16x8 xo;
#pragma unroll
  for (int j = 0; j < 8; ++j) {
    int cidx = lane * 8 + j;
    float t = rbf(bf2f((u16)xv[j]) + rbf(mod[2 * D_ + cidx] * rbf(a[j])));
    v[j] = t; s += t; xo[j] = (short)f2bf(t);
  }
  *reinterpret_cast<bf16x8*>(x1o + (long)row * D_ + lane * 8) = xo;
  float mean = wave_sum(s) * (1.f / D_);
  float q = 0.f;
#pragma unroll
  for (int j = 0; j < 8; ++j) { float d = v[j] - mean; q += d * d; }
  float rstd = rsqrtf(wave_sum(q) * (1.f / D_) + 1e-5f);
  bf16x8 o;
#pragma unroll
  for (int j = 0; j < 8; ++j) {
    int cidx = lane * 8 + j;
    o[j] = (short)f2bf((v[j] - mean) * rstd * (1.f + mod[4 * D_ + cidx]) + mod[3 * D_ + cidx]);
  }
  *reinterpret_cast<bf16x8*>(h2 + (long)row * D_ + lane * 8) = o;
}

__global__ __launch_bounds__(256) void k_ln_z2(const u16* __restrict__ z, u16* __restrict__ z2n) {
  const int lane = threadIdx.x & 63, row = blockIdx.x * 4 + (threadIdx.x >> 6);
  float v[16]; float s = 0.f; const bool act = lane < 56;
  bf16x8 c0 = {}, c1 = {};
  if (act) {
    c0 = *reinterpret_cast<const bf16x8*>(z + (long)row * F_ + FH + lane * 16);
    c1 = *reinterpret_cast<const bf16x8*>(z + (long)row * F_ + FH + lane * 16 + 8);
  }
#pragma unroll
  for (int j = 0; j < 8; ++j) { v[j] = bf2f((u16)c0[j]); v[8 + j] = bf2f((u16)c1[j]); s += v[j] + v[8 + j]; }
  float mean = wave_sum(s) * (1.f / FH);
  float q = 0.f;
  if (act) {
#pragma unroll
    for (int j = 0; j < 16; ++j) { float d = v[j] - mean; q += d * d; }
  }
  float rstd = rsqrtf(wave_sum(q) * (1.f / FH) + 1e-5f);
  if (act) {
    bf16x8 o0, o1;
#pragma unroll
    for (int j = 0; j < 8; ++j) { o0[j] = (short)f2bf((v[j] - mean) * rstd); o1[j] = (short)f2bf((v[8 + j] - mean) * rstd); }
    *reinterpret_cast<bf16x8*>(z2n + (long)row * FH + lane * 16) = o0;
    *reinterpret_cast<bf16x8*>(z2n + (long)row * FH + lane * 16 + 8) = o1;
  }
}

constexpr int NW = 8, QBLK = 32, KVBLK = 64;
constexpr float SCALE = 0.088388347648318440f;
constexpr float THR = 8.f;
constexpr size_t SHM_V = KVBLK * DH * 2, SHM_K = KVBLK * DH * 2, SHM_ATTN = 2 * SHM_V + 2 * SHM_K + NW * 64 * 4;
#define KSWZ(row, colB) ((row) * 256 + ((colB) ^ (((row) & 7) << 4)))
#define SBAR() __builtin_amdgcn_sched_barrier(0)

__device__ __forceinline__ void partialSM(f32x16& p0, f32x16& p1, float& m_reg, float& mn, float& alpha) {
  constexpr float C = SCALE * 1.4426950408889634f;
  float pmax = p0[0];
  for (int r = 1; r < 16; ++r) pmax = fmaxf(pmax, p0[r]);
  for (int r = 0; r < 16; ++r) pmax = fmaxf(pmax, p1[r]);
  { auto rr = __builtin_amdgcn_permlane32_swap(__float_as_uint(pmax), __float_as_uint(pmax), false, false);
    pmax = fmaxf(__uint_as_float(rr[0]), __uint_as_float(rr[1])); }
  if (__builtin_expect(__all(pmax - m_reg <= THR / SCALE), 1)) { mn = m_reg; alpha = 1.f; }
  else { mn = fmaxf(m_reg, pmax); alpha = __builtin_amdgcn_exp2f((m_reg - mn) * C); m_reg = mn; }
  float mnC = -mn * C;
  for (int r = 0; r < 16; ++r) p0[r] = fmaf(p0[r], C, mnC);
  for (int r = 0; r < 16; ++r) p1[r] = fmaf(p1[r], C, mnC);
  for (int r = 0; r < 16; ++r) p0[r] = __builtin_amdgcn_exp2f(p0[r]);
}
__device__ __forceinline__ void finishSM(f32x16& p0, f32x16& p1, float alpha, float& l_reg, bf16x8& pa0, bf16x8& pa1, bf16x8& pa2, bf16x8& pa3) {
  for (int r = 0; r < 16; ++r) p1[r] = __builtin_amdgcn_exp2f(p1[r]);
  float ps = 0;
  for (int r = 0; r < 16; ++r) ps += p0[r];
  for (int r = 0; r < 16; ++r) ps += p1[r];
  { auto rr = __builtin_amdgcn_permlane32_swap(__float_as_uint(ps), __float_as_uint(ps), false, false);
    ps = __uint_as_float(rr[0]) + __uint_as_float(rr[1]); }
  l_reg = l_reg * alpha + ps;
#define PK4(P, BASE, OUT) do { unsigned a0 = cvtpk(P[BASE + 0], P[BASE + 1]), a1 = cvtpk(P[BASE + 2], P[BASE + 3]);   \
    unsigned b0 = cvtpk(P[BASE + 4], P[BASE + 5]), b1 = cvtpk(P[BASE + 6], P[BASE + 7]);                              \
    auto r0 = __builtin_amdgcn_permlane32_swap(a0, b0, false, false); auto r1 = __builtin_amdgcn_permlane32_swap(a1, b1, false, false); \
    u32x4 w = {r0[0], r1[0], r0[1], r1[1]}; OUT = *reinterpret_cast<bf16x8*>(&w); } while (0)
  PK4(p0, 0, pa0); PK4(p0, 8, pa1); PK4(p1, 0, pa2); PK4(p1, 8, pa3);
#undef PK4
}
__device__ __forceinline__ void qkt(f32x16& p0, f32x16& p1, const u16* Ks, const bf16x8* qr, int r32, int hi) {
  p0 = f32x16{}; p1 = f32x16{};
  for (int d0 = 0; d0 < 8; ++d0) { int cb = (d0 * 16 + hi * 8) * 2;
    bf16x8 b0 = *reinterpret_cast<const bf16x8*>((const char*)Ks + KSWZ(r32, cb));
    bf16x8 b1 = *reinterpret_cast<const bf16x8*>((const char*)Ks + KSWZ(32 + r32, cb));
    p0 = __builtin_amdgcn_mfma_f32_32x32x16_bf16(b0, qr[d0], p0, 0, 0, 0);
    p1 = __builtin_amdgcn_mfma_f32_32x32x16_bf16(b1, qr[d0], p1, 0, 0, 0); }
}
__device__ __forceinline__ int v_st(int k, int c) { const int kk = (k & ~0xC) | ((k & 4) << 1) | ((k & 8) >> 1); return ((kk >> 3) * 4 + (c >> 5)) * 512 + ((kk & 7) * 32 + (c & 31)) * 2; }
__device__ __forceinline__ int v_rd_base(int lane) { return ((lane & 3) << 3) | (((lane >> 2) & 3) << 6) | (((lane >> 4) & 1) << 5) | (((lane >> 5) & 1) << 8); }
constexpr int v_rd_off(int d0, int ks, int half) { return d0 * 512 + ks * 4096 + half * 2048; }
template <int OFF> __device__ __forceinline__ s16x4 tr_read(int vb) {
  s16x4 r; asm volatile("ds_read_b64_tr_b16 %0, %1 offset:%2" : "=&v"(r) : "v"(vb), "i"(OFF) : "memory"); return r;
}
template <int D0> __device__ __forceinline__ void pv_one(f32x16& od, int vb, bf16x8 pa0, bf16x8 pa1, bf16x8 pa2, bf16x8 pa3) {
  const s16x4 l0 = tr_read<v_rd_off(D0, 0, 0)>(vb), h0 = tr_read<v_rd_off(D0, 0, 1)>(vb), l1 = tr_read<v_rd_off(D0, 1, 0)>(vb), h1 = tr_read<v_rd_off(D0, 1, 1)>(vb);
  const s16x4 l2 = tr_read<v_rd_off(D0, 2, 0)>(vb), h2 = tr_read<v_rd_off(D0, 2, 1)>(vb), l3 = tr_read<v_rd_off(D0, 3, 0)>(vb), h3 = tr_read<v_rd_off(D0, 3, 1)>(vb);
  asm volatile("s_waitcnt lgkmcnt(0)" ::: "memory"); SBAR();
#define PK(L, H) (bf16x8){L[0], L[1], L[2], L[3], H[0], H[1], H[2], H[3]}
  od = __builtin_amdgcn_mfma_f32_32x32x16_bf16(pa0, PK(l0, h0), od, 0, 0, 0);
  od = __builtin_amdgcn_mfma_f32_32x32x16_bf16(pa1, PK(l1, h1), od, 0, 0, 0);
  od = __builtin_amdgcn_mfma_f32_32x32x16_bf16(pa2, PK(l2, h2), od, 0, 0, 0);
  od = __builtin_amdgcn_mfma_f32_32x32x16_bf16(pa3, PK(l3, h3), od, 0, 0, 0);
#undef PK
}
__device__ __forceinline__ void pv_d0(f32x16* o, int vb, bf16x8 pa0, bf16x8 pa1, bf16x8 pa2, bf16x8 pa3) {
  pv_one<0>(o[0], vb, pa0, pa1, pa2, pa3); pv_one<1>(o[1], vb, pa0, pa1, pa2, pa3); pv_one<2>(o[2], vb, pa0, pa1, pa2, pa3); pv_one<3>(o[3], vb, pa0, pa1, pa2, pa3);
}

constexpr size_t SHM_FUSED = 131072;
__global__ __launch_bounds__(512, 1) void k_attn(const u16* __restrict__ X, const u16* __restrict__ Wq, const float* __restrict__ mod,
                                                 const u16* __restrict__ Kc, const u16* __restrict__ Vc, u16* __restrict__ O) {
  extern __shared__ __attribute__((aligned(16))) char lds[];
  const int b = blockIdx.x;
  const int head = (b & 7) + 8 * ((b >> 3) & 3), qb = b >> 5;
  const u16* Kh = Kc + (long)head * S_ * DH;
  const u16* Vh = Vc + (long)head * S_ * DH;
  u16* Ob = O + (long)qb * 256 * HD + head * DH;
  const int tid = threadIdx.x, wid = tid >> 6, lane = tid & 63, r32 = lane & 31, hi = lane >> 5;
  u16* V_lds = (u16*)lds; u16* K_lds = (u16*)(lds + 2 * SHM_V);
  float* ws = (float*)(lds + 2 * SHM_V + 2 * SHM_K) + wid * 64; float* li_l = ws; float* al_l = ws + 32;
  float m_reg = -1e30f, l_reg = 0; f32x16 o[4] = {}; bf16x8 qr[8];
  {
    const unsigned l0 = (unsigned)(uintptr_t)lds + __builtin_amdgcn_readfirstlane(wid) * 1024;
    const u16* xr = X + (long)(qb * 256 + wid * QBLK + r32) * D_ + hi * 64;
    bf16x8 xf[32];
#pragma unroll
    for (int ks = 0; ks < 32; ++ks) xf[ks] = *reinterpret_cast<const bf16x8*>(xr + (ks >> 3) * 128 + (ks & 7) * 8);
#pragma unroll
    for (int i = 0; i < 16; ++i) {
      const int qt = i >> 2, panel = i & 3, row = qt * 128 + (tid >> 2), ch = tid & 3;
      glds16(Wq + (long)row * HD + head * DH + panel * 32 + ch * 8, l0 + panel * 32768 + qt * 8192);
    }
    f32x16 qa[4] = {};
    const char* wb = lds + (64 * hi + ((lane & 15) >> 2)) * 64 + ((lane >> 4) & 1) * 32 + (lane & 3) * 8;
#pragma unroll
    for (int qt = 0; qt < 4; ++qt) {
      if (qt == 0) wait_vm<12>(); else if (qt == 1) wait_vm<8>(); else if (qt == 2) wait_vm<4>(); else wait_vm<0>();
      __builtin_amdgcn_s_barrier();
#pragma unroll
      for (int k8 = 0; k8 < 8; ++k8) {
        const int ks = qt * 8 + k8, k0 = qt * 128 + k8 * 8;
#pragma unroll
        for (int bl = 0; bl < 4; ++bl) {
          const char* pb = wb + bl * 32768 + k0 * 64;
          s16x4 lo = tr_read_b(pb), hh = tr_read_b(pb + 256);
          bf16x8 af = (bf16x8){lo[0], lo[1], lo[2], lo[3], hh[0], hh[1], hh[2], hh[3]};
          qa[bl] = __builtin_amdgcn_mfma_f32_32x32x16_bf16(af, xf[ks], qa[bl], 0, 0, 0);
        }
      }
    }
    SBAR(); asm volatile("s_nop 15\n\ts_nop 15" ::: "memory"); SBAR();
#define PKQ(P, BASE, OUT) do { unsigned a0 = cvtpk(P[BASE + 0], P[BASE + 1]), a1 = cvtpk(P[BASE + 2], P[BASE + 3]);   \
    unsigned b0 = cvtpk(P[BASE + 4], P[BASE + 5]), b1 = cvtpk(P[BASE + 6], P[BASE + 7]);                              \
    auto r0 = __builtin_amdgcn_permlane32_swap(a0, b0, false, false); auto r1 = __builtin_amdgcn_permlane32_swap(a1, b1, false, false); \
    u32x4 w = {r0[0], r1[0], r0[1], r1[1]}; OUT = *reinterpret_cast<bf16x8*>(&w); } while (0)
#pragma unroll
    for (int bl = 0; bl < 4; ++bl) { PKQ(qa[bl], 0, qr[2 * bl]); PKQ(qa[bl], 8, qr[2 * bl + 1]); }
#undef PKQ
    __syncthreads();
  }
  const int sr = tid >> 4, sc = (tid & 15) * 8, vst0 = v_st(sr, sc), vst1 = v_st(32 + sr, sc);
  const int vb0 = (int)(uintptr_t)V_lds + v_rd_base(lane);
  struct { bf16x8 vs0, vs1, ks0, ks1; } sr_[2];
#define LD8(p) (*reinterpret_cast<const bf16x8*>(p))
#define SLOAD(i, k0) do { sr_[i].vs0 = LD8(&Vh[(long)((k0) + sr) * DH + sc]); sr_[i].vs1 = LD8(&Vh[(long)((k0) + 32 + sr) * DH + sc]); \
    sr_[i].ks0 = LD8(&Kh[(long)((k0) + sr) * DH + sc]); sr_[i].ks1 = LD8(&Kh[(long)((k0) + 32 + sr) * DH + sc]); } while (0)
#define SWRITE(b, i) do { *(bf16x8*)((char*)V_lds + (b) * SHM_V + vst0) = sr_[i].vs0;          \
    *(bf16x8*)((char*)V_lds + (b) * SHM_V + vst1) = sr_[i].vs1; int kc = sc * 2;               \
    *(bf16x8*)((char*)K_lds + (b) * SHM_K + KSWZ(sr, kc)) = sr_[i].ks0;                       \
    *(bf16x8*)((char*)K_lds + (b) * SHM_K + KSWZ(32 + sr, kc)) = sr_[i].ks1; } while (0)
#define SWAIT() asm volatile("s_waitcnt vmcnt(4)" ::: "memory")
#define RESC(a) do { if (__any((a) < 1.f)) { if (hi == 0) al_l[r32] = (a); asm volatile("s_waitcnt lgkmcnt(0)" ::: "memory"); \
    for (int d = 0; d < 4; ++d) for (int r = 0; r < 16; ++r) o[d][r] *= al_l[crow(r, hi)]; } } while (0)
  f32x16 pA0, pA1, pB0, pB1; float mnA, mnB, alA, alB; bf16x8 pa0, pa1, pa2, pa3; constexpr int NT = S_ / KVBLK;
  constexpr int SE = 0, SO = 1;
  SLOAD(SE, 0); asm volatile("s_waitcnt vmcnt(0)" ::: "memory"); SWRITE(0, SE); __syncthreads();
  qkt(pA0, pA1, K_lds, qr, r32, hi); partialSM(pA0, pA1, m_reg, mnA, alA);
  SLOAD(SO, KVBLK); SLOAD(SE, 2 * KVBLK);
  SWAIT(); SWRITE(1, SO); __syncthreads();
  for (int j = 1; j + 1 < NT; j += 2) {
    SBAR(); qkt(pB0, pB1, (u16*)((char*)K_lds + SHM_K), qr, r32, hi);
    finishSM(pA0, pA1, alA, l_reg, pa0, pa1, pa2, pa3); SBAR();
    SLOAD(SO, (j + 2) * KVBLK); SBAR();
    pv_d0(o, vb0, pa0, pa1, pa2, pa3); partialSM(pB0, pB1, m_reg, mnB, alB);
    __syncthreads(); SWAIT(); SWRITE(0, SE);
    RESC(alB); __syncthreads();
    SBAR(); qkt(pA0, pA1, K_lds, qr, r32, hi);
    finishSM(pB0, pB1, alB, l_reg, pa0, pa1, pa2, pa3); SBAR();
    if (j + 3 < NT) SLOAD(SE, (j + 3) * KVBLK); SBAR();
    pv_d0(o, vb0 + (int)SHM_V, pa0, pa1, pa2, pa3); partialSM(pA0, pA1, m_reg, mnA, alA);
    __syncthreads(); SWAIT(); SWRITE(1, SO);
    RESC(alA); __syncthreads();
  }
  SBAR(); qkt(pB0, pB1, (u16*)((char*)K_lds + SHM_K), qr, r32, hi);
  finishSM(pA0, pA1, alA, l_reg, pa0, pa1, pa2, pa3); SBAR();
  pv_d0(o, vb0, pa0, pa1, pa2, pa3); partialSM(pB0, pB1, m_reg, mnB, alB);
  __syncthreads(); RESC(alB);
  finishSM(pB0, pB1, alB, l_reg, pa0, pa1, pa2, pa3); SBAR();
  pv_d0(o, vb0 + (int)SHM_V, pa0, pa1, pa2, pa3);
  if (hi == 0) li_l[r32] = l_reg; asm volatile("s_waitcnt lgkmcnt(0)" ::: "memory");
  float rli[16];
#pragma unroll
  for (int r = 0; r < 16; ++r) rli[r] = __builtin_amdgcn_rcpf(li_l[crow(r, hi)]);
  u16* Ow = Ob + (long)(wid * QBLK) * HD;
#pragma unroll
  for (int r = 0; r < 16; ++r) { int orow = crow(r, hi);
#pragma unroll
    for (int d0 = 0; d0 < 4; ++d0) Ow[(long)orow * HD + d0 * 32 + r32] = f2bf(o[d0][r] * rli[r]); }
#undef SLOAD
#undef SWRITE
#undef SWAIT
#undef RESC
}

extern "C" void kernel_launch(void* const* d_in, const int* in_sizes, int n_in, void* d_out, int out_size, void* d_ws, size_t ws_size,
                              hipStream_t stream) {
  const u16* x = (const u16*)d_in[0];
  const u16* kc = (const u16*)d_in[1];
  const u16* vc = (const u16*)d_in[2];
  const u16* wq = (const u16*)d_in[3];
  const u16* wo = (const u16*)d_in[4];
  const u16* w_in = (const u16*)d_in[6];
  const u16* w_sp = (const u16*)d_in[7];
  const u16* b_sp = (const u16*)d_in[8];
  const u16* w_out = (const u16*)d_in[9];
  const u16* c = (const u16*)d_in[10];
  const u16* w_mod = (const u16*)d_in[11];
  u16* out = (u16*)d_out;

  char* ws = (char*)d_ws;
  size_t off = 0;
  auto carve = [&](size_t bytes) { void* p = ws + off; off += (bytes + 255) / 256 * 256; return p; };
  float* mod = (float*)carve(3072 * 4);
  u16* h1 = (u16*)carve((size_t)S_ * D_ * 2);
  u16* q = (u16*)carve((size_t)S_ * HD * 2);
  u16* o = (u16*)carve((size_t)S_ * HD * 2);
  float* part = (float*)carve((size_t)4 * S_ * D_ * 4);
  u16* x1 = (u16*)carve((size_t)S_ * D_ * 2);
  u16* h2 = (u16*)carve((size_t)S_ * D_ * 2);
  u16* z = (u16*)carve((size_t)S_ * F_ * 2);
  u16* z2n = (u16*)carve((size_t)S_ * FH * 2);
  u16* u = (u16*)carve((size_t)S_ * FH * 2);

  constexpr int GS = 2 * (128 * 64 * 2 + 64 * 128 * 2);
  hipFuncSetAttribute((const void*)k_gemm<64, 128, 2, 2, EPI_BF16, 3>, hipFuncAttributeMaxDynamicSharedMemorySize, 3 * 24576);
  hipFuncSetAttribute((const void*)k_gemm<64, 128, 2, 2, EPI_F32, 3>, hipFuncAttributeMaxDynamicSharedMemorySize, 3 * 24576);
  hipFuncSetAttribute((const void*)k_gemm<64, 128, 2, 2, EPI_GELU, 3>, hipFuncAttributeMaxDynamicSharedMemorySize, 3 * 24576);
  hipFuncSetAttribute((const void*)k_gemm<64, 128, 2, 2, EPI_SP, 4>, hipFuncAttributeMaxDynamicSharedMemorySize, 4 * 24576);
  hipFuncSetAttribute((const void*)k_gemm<64, 64, 2, 2, EPI_OUT, 4>, hipFuncAttributeMaxDynamicSharedMemorySize, 4 * 16384);
  hipFuncSetAttribute((const void*)k_attn, hipFuncAttributeMaxDynamicSharedMemorySize, (int)SHM_FUSED);

  k_mod<<<48, 256, 0, stream>>>(c, w_mod, mod);
  k_ln1<<<S_ / 4, 256, 0, stream>>>(x, mod, h1);
  k_attn<<<256, 512, SHM_FUSED, stream>>>(h1, wq, mod, kc, vc, o);
  { EpiArgs ea{part, nullptr, nullptr, nullptr, D_, 0};
    k_gemm<64, 128, 2, 2, EPI_F32, 3><<<dim3(D_ / 128, S_ / 64, 4), 256, 3 * 24576, stream>>>(o, wo, ea, HD, D_, HD, HD / 4); }
  k_resid_ln2<4><<<S_ / 4, 256, 0, stream>>>(x, part, mod, x1, h2);
  { EpiArgs ea{z, nullptr, nullptr, nullptr, F_, 0};
    k_gemm<64, 128, 2, 2, EPI_GELU, 3><<<dim3(F_ / 128, S_ / 64, 1), 256, 3 * 24576, stream>>>(h2, w_in, ea, D_, F_, D_, D_); }
  k_ln_z2<<<S_ / 4, 256, 0, stream>>>(z, z2n);
  { EpiArgs ea{u, z, b_sp, nullptr, FH, 0};
    k_gemm<64, 128, 2, 2, EPI_SP, 4><<<dim3(FH / 128, S_ / 64, 1), 256, 4 * 24576, stream>>>(w_sp, z2n, ea, S_, FH, S_, S_); }
  { EpiArgs ea{out, x1, nullptr, mod + 5 * D_, D_, 0};
    k_gemm<64, 64, 2, 2, EPI_OUT, 4><<<dim3(D_ / 64, S_ / 64, 1), 256, 4 * 16384, stream>>>(u, w_out, ea, FH, D_, FH, FH); }
}
```
